```python
import torch

def _softmax(x, axis=-1):
    return torch.softmax(x, dim=axis)
def _gelu(x):
    return torch.nn.functional.gelu(x, approximate="tanh")
def _silu(x):
    return torch.nn.functional.silu(x)
def _sigmoid(x):
    return torch.sigmoid(x)
def _layernorm(x, axis=-1, eps=1e-5):
    m = x.mean(dim=axis, keepdim=True)
    v = x.var(dim=axis, keepdim=True, unbiased=False)
    return (x - m) / torch.sqrt(v + eps)
def _scatter_add(tgt, idx, src, axis=0):
    return tgt.clone().index_add_(axis, idx, src)
def _segment_sum(src, seg, n):
    out = torch.zeros((n,) + src.shape[1:], dtype=src.dtype)
    return out.index_add_(0, seg, src)
def _pad(x, pw):
    flat = tuple(v for pair in reversed(pw) for v in pair)
    return torch.nn.functional.pad(x, flat)

kernel_name = 'gather_scatter__cheap_equivariant_egnn__graph'

def setup_inputs():

    _w = (lambda a: (lambda t: t.to(torch.bfloat16) if t.is_floating_point() else t)(torch.as_tensor(a)))
    N, deg, D, De = 10000, 64, 64, max(4, 1)
    E = N * deg
    return {
        "h": _w((lambda *s: torch.randn(*s, dtype=torch.bfloat16) * 0.2)(N, D)),
        "pos": _w((lambda *s: torch.randn(*s, dtype=torch.bfloat16) * 0.8)(N, 3)),
        "e_attr": _w((lambda *s: torch.randn(*s, dtype=torch.bfloat16) * 0.8)(E, De)),
        "src": _w((lambda lo, hi, *s: torch.randint(lo, hi, s))(0, N, E).to(torch.int64)),
        "dst": _w((lambda lo, hi, *s: torch.randint(lo, hi, s))(0, N, E).to(torch.int64)),
        "w_e": _w(0.1 * (lambda *s: torch.randn(*s, dtype=torch.bfloat16) * 0.8)(2 * D + 1 + De, D)),
        "w_x": _w(0.1 * (lambda *s: torch.randn(*s, dtype=torch.bfloat16) * 0.2)(D, 1)),
        "w_h": _w(0.1 * (lambda *s: torch.randn(*s, dtype=torch.bfloat16) * 0.2)(2 * D, D)),
    }

def reference(h, pos, e_attr, src, dst, w_e, w_x, w_h):
    N, D = h.shape[0], h.shape[1]
    diff = pos[dst] - pos[src]
    dist2 = torch.sum(diff * diff, axis=-1, keepdims=True)
    e_in = torch.concatenate([h[dst], h[src], dist2, e_attr], axis=-1)
    m_ij = _silu(e_in @ w_e)
    coord_scale = m_ij @ w_x
    pos_new = pos + _segment_sum(diff * coord_scale, dst, N)
    m_i = _segment_sum(m_ij, dst, N)
    h_new = h + _silu(torch.concatenate([h, m_i], axis=-1) @ w_h)
    return h_new, pos_new
```

```cpp
#include <hip/hip_runtime.h>
#include <hip/hip_bf16.h>
#include <stdint.h>
typedef unsigned short u16;
__device__ __forceinline__ float bf2f(u16 x){ return __uint_as_float(((unsigned)x)<<16); }
__device__ __forceinline__ u16 f2bf(float f){ __hip_bfloat16 b = __float2bfloat16(f); return *(u16*)&b; }
__device__ __forceinline__ float rbf(float f){ return bf2f(f2bf(f)); }

__global__ void k_pre(const u16* h, const u16* w_e, float* A, float* B, float* macc, float* pacc, int N){
  int n = blockIdx.x; int c = threadIdx.x;
  float a=0,b=0;
  for(int k=0;k<64;k++){ float hv=bf2f(h[n*64+k]); a+=hv*bf2f(w_e[k*64+c]); b+=hv*bf2f(w_e[(64+k)*64+c]); }
  A[n*64+c]=a; B[n*64+c]=b; macc[n*64+c]=0; if(c<4) pacc[n*4+c]=0;
}
__global__ void k_edge(const u16* pos, const u16* ea, const int* src, const int* dst, const u16* w_e, const u16* w_x,
   const float* A, const float* B, float* macc, float* pacc, int E){
  int e = blockIdx.x*4 + (threadIdx.x>>6); int c = threadIdx.x&63;
  if(e>=E) return;
  int s=src[e], d=dst[e];
  float dx=rbf(bf2f(pos[d*3])-bf2f(pos[s*3])), dy=rbf(bf2f(pos[d*3+1])-bf2f(pos[s*3+1])), dz=rbf(bf2f(pos[d*3+2])-bf2f(pos[s*3+2]));
  float d2=rbf(rbf(dx*dx)+rbf(dy*dy)+rbf(dz*dz));
  float x=A[d*64+c]+B[s*64+c]+d2*bf2f(w_e[128*64+c]);
  for(int k=0;k<4;k++) x+=bf2f(ea[e*4+k])*bf2f(w_e[(129+k)*64+c]);
  float m=x/(1.f+__expf(-x));
  m=rbf(m);
  float cs=m*bf2f(w_x[c]);
  for(int o=32;o;o>>=1) cs+=__shfl_xor(cs,o);
  atomicAdd(&macc[d*64+c],m);
  if(c<3) atomicAdd(&pacc[d*4+c], cs*(c==0?dx:c==1?dy:dz));
}
__global__ void k_node(const u16* h, const u16* pos, const u16* w_h, const float* macc, const float* pacc, u16* out, int N){
  int n = blockIdx.x; int c = threadIdx.x;
  float a=0;
  for(int k=0;k<64;k++){ a+=bf2f(h[n*64+k])*bf2f(w_h[k*64+c]) + rbf(macc[n*64+k])*bf2f(w_h[(64+k)*64+c]); }
  float s=a/(1.f+__expf(-a));
  out[n*64+c]=f2bf(bf2f(h[n*64+c])+s);
  if(c<3) out[N*64+n*3+c]=f2bf(bf2f(pos[n*3+c])+pacc[n*4+c]);
}
extern "C" void kernel_launch(void* const* d_in, const int* in_sizes, int n_in,
                              void* d_out, int out_size, void* d_ws, size_t ws_size,
                              hipStream_t stream) {
  const u16* h=(const u16*)d_in[0]; const u16* pos=(const u16*)d_in[1]; const u16* ea=(const u16*)d_in[2];
  const int* src=(const int*)d_in[3]; const int* dst=(const int*)d_in[4];
  const u16* w_e=(const u16*)d_in[5]; const u16* w_x=(const u16*)d_in[6]; const u16* w_h=(const u16*)d_in[7];
  int N=in_sizes[0]/64, E=in_sizes[3];
  float* A=(float*)d_ws; float* B=A+N*64; float* macc=B+N*64; float* pacc=macc+N*64;
  k_pre<<<N,64,0,stream>>>(h,w_e,A,B,macc,pacc,N);
  k_edge<<<(E+3)/4,256,0,stream>>>(pos,ea,src,dst,w_e,w_x,A,B,macc,pacc,E);
  k_node<<<N,64,0,stream>>>(h,pos,w_h,macc,pacc,(u16*)d_out,N);
}
```

```cpp
#include <hip/hip_runtime.h>
#include <hip/hip_bf16.h>
#include <stdint.h>

typedef unsigned short u16;
typedef unsigned char u8;
typedef unsigned u32;
typedef unsigned long long u64;
typedef u32 u32x2 __attribute__((ext_vector_type(2)));
typedef u32 u32x3 __attribute__((ext_vector_type(3)));
typedef u32 u32x4 __attribute__((ext_vector_type(4)));
typedef float f32x2 __attribute__((ext_vector_type(2)));
typedef float f32x4 __attribute__((ext_vector_type(4)));
typedef float f32x16 __attribute__((ext_vector_type(16)));
typedef __bf16 bf16x2 __attribute__((ext_vector_type(2)));
typedef __bf16 bf16x8 __attribute__((ext_vector_type(8)));
typedef short s16x4 __attribute__((ext_vector_type(4)));
#define DI __device__ __forceinline__
#define LDSP(T, a) ((__attribute__((address_space(3))) T*)(a))

constexpr int NN = 10000, EE = 640000;
constexpr int NPB = 40, NBK = 250;
constexpr int CH = 2560, NCH = 250;
constexpr int QCAP = 640;
constexpr float LOG2E = 1.4426950408889634f, LN2 = 0.6931471805599453f;

constexpr size_t WS_REC = 0;
constexpr size_t WS_TAB = WS_REC + (size_t)EE * 12;
constexpr size_t WS_BP = WS_TAB + (size_t)NCH * 1024;
constexpr size_t WS_AP = WS_BP + (size_t)NN * 128;
constexpr size_t WS_P4 = WS_AP + (size_t)NN * 128;
constexpr size_t WS_WX = WS_P4 + 81920;
constexpr size_t WS_WH = WS_WX + 1024;
constexpr size_t WS_END = WS_WH + 16384;

#define ST(k, i)

DI float bf2f(u16 x) { return __uint_as_float(((u32)x) << 16); }
DI u32 pk2(float a, float b) { f32x2 f = {a, b}; return __builtin_bit_cast(u32, __builtin_convertvector(f, bf16x2)); }
DI float lo2f(u32 x) { return __uint_as_float(x << 16); }
DI float hi2f(u32 x) { return __uint_as_float(x & 0xFFFF0000u); }
DI f32x16 mfma32(u32x4 a, u32x4 b, f32x16 c) {
  return __builtin_amdgcn_mfma_f32_32x32x16_bf16(__builtin_bit_cast(bf16x8, a), __builtin_bit_cast(bf16x8, b), c, 0, 0, 0); }
DI f32x4 mfma16(u32x4 a, u32x4 b, f32x4 c) {
  return __builtin_amdgcn_mfma_f32_16x16x32_bf16(__builtin_bit_cast(bf16x8, a), __builtin_bit_cast(bf16x8, b), c, 0, 0, 0); }
DI u32x2 trread(u32 a) { return __builtin_bit_cast(u32x2, __builtin_amdgcn_ds_read_tr16_b64_v4i16(LDSP(s16x4, a))); }
template <typename T> DI T ldsr(u32 a) { return *LDSP(T, a); }
template <typename T> DI void ldsw(u32 a, T v) { *LDSP(T, a) = v; }

constexpr int K1_HIST = 0, K1_W = 1024, K1_H = K1_W + 16384, K1_REC = K1_H + 40 * 144, K1_LDS = K1_REC + CH * 12;

__global__ __launch_bounds__(640) void k1_bin(const u16* __restrict__ h, const u16* __restrict__ pos, const u32x4* __restrict__ ea,
                                              const u32x4* __restrict__ src, const u32x4* __restrict__ dst,
                                              const u16* __restrict__ w_e, const u16* __restrict__ w_h, char* __restrict__ ws) {
  __shared__ __attribute__((aligned(16))) char smem[K1_LDS];
  const u32 L = (u32)(size_t)smem;
  const int tid = threadIdx.x, c = blockIdx.x;
  ST(0, 0);
  if (c >= NCH) {
    const int id = (c - NCH) * 640 + tid;
    if (id < 1024) {
      const int lane = id & 63, ks = (id >> 6) & 3, u = (id >> 8) & 1, cp = id >> 9;
      const int col = 32 * cp + 2 * (lane & 15) + u, k0 = 32 * ks + 8 * (lane >> 4);
      u32x4 v;
#pragma unroll
      for (int j = 0; j < 4; ++j) v[j] = (u32)w_h[(k0 + 2 * j) * 64 + col] | ((u32)w_h[(k0 + 2 * j + 1) * 64 + col] << 16);
      ((u32x4*)(ws + WS_WH))[id] = v;
    } else if (id < 1088) {
      const int ch = id - 1024;
      u32x4 v;
      v[0] = pk2(LOG2E * bf2f(w_e[129 * 64 + ch]), LOG2E * bf2f(w_e[130 * 64 + ch]));
      v[1] = pk2(LOG2E * bf2f(w_e[131 * 64 + ch]), LOG2E * bf2f(w_e[132 * 64 + ch]));
      v[2] = (u32)w_e[128 * 64 + ch];
      v[3] = 0;
      ((u32x4*)(ws + WS_WX))[ch] = v;
    }
    return;
  }
  const int lane = tid & 63, w = __builtin_amdgcn_readfirstlane(tid >> 6);
  const u32x4 d4 = dst[c * 640 + tid];
  const u32x4 wv0 = ((const u32x4*)w_e)[tid];
  u32x4 wv1, hv;
  if (tid < 384) wv1 = ((const u32x4*)w_e)[640 + tid];
  if (tid < 320) hv = ((const u32x4*)h)[c * 320 + tid];
  u16 px, py, pz;
  if (tid >= 576 && tid < 616) { const u16* pp = pos + (c * NPB + tid - 576) * 3; px = pp[0]; py = pp[1]; pz = pp[2]; }
  const u32x4 s4 = src[c * 640 + tid];
  const u32x4 e0 = ea[c * 1280 + 2 * tid], e1 = ea[c * 1280 + 2 * tid + 1];
  if (tid < 256) ldsw<u32>(L + K1_HIST + 4 * tid, 0u);
  __syncthreads();
  u32 bk[4], rank[4];
#pragma unroll
  for (int i = 0; i < 4; ++i) {
    bk[i] = d4[i] / (u32)NPB;
    rank[i] = __hip_atomic_fetch_add(LDSP(u32, L + K1_HIST + 4 * bk[i]), 1u, __ATOMIC_RELAXED, __HIP_MEMORY_SCOPE_WORKGROUP);
  }
  ldsw<u32x4>(L + K1_W + 16 * tid, wv0);
  if (tid < 384) ldsw<u32x4>(L + K1_W + 10240 + 16 * tid, wv1);
  if (tid < 320) ldsw<u32x4>(L + K1_H + (tid >> 3) * 144 + (tid & 7) * 16, hv);
  if (tid >= 576 && tid < 616) { u32x2 p = {(u32)px | ((u32)py << 16), (u32)pz}; ((u32x2*)(ws + WS_P4))[c * NPB + tid - 576] = p; }
  __syncthreads();
  ST(0, 1);
  if (w == 0) {
    const u32x4 cn = ldsr<u32x4>(L + K1_HIST + 16 * lane);
    const u32 tot = cn[0] + cn[1] + cn[2] + cn[3];
    u32 x = tot;
#pragma unroll
    for (int o = 1; o < 64; o <<= 1) { const u32 y = __shfl_up(x, o); if (lane >= o) x += y; }
    u32x4 b; b[0] = x - tot; b[1] = b[0] + cn[0]; b[2] = b[1] + cn[1]; b[3] = b[2] + cn[2];
    ldsw<u32x4>(L + K1_HIST + 16 * lane, b);
    ((u32x4*)(ws + WS_TAB))[c * 64 + lane] = b | (cn << 16);
  } else if (w >= 2) {
    const int ww = w - 2, kind = ww >> 2, rt = (ww >> 1) & 1, t = ww & 1;
    const int r = lane & 31, hh = lane >> 5;
    const int row = min(32 * rt + r, NPB - 1);
    const u32 ha = L + K1_H + row * 144 + 16 * hh;
    const u32 wa = L + K1_W + (kind * 64 + 8 * hh + ((lane >> 2) & 3)) * 128 + (32 * t + 16 * ((lane >> 4) & 1) + 4 * (lane & 3)) * 2;
    f32x16 acc;
#pragma unroll
    for (int i = 0; i < 16; ++i) acc[i] = 0.f;
#pragma unroll
    for (int s = 0; s < 4; ++s) {
      const u32x4 hf = ldsr<u32x4>(ha + 32 * s);
      const u32x2 w0 = trread(wa + s * 2048), w1 = trread(wa + s * 2048 + 512);
      const u32x4 wf = {w0[0], w0[1], w1[0], w1[1]};
      acc = kind == 0 ? mfma32(hf, wf, acc) : mfma32(wf, hf, acc);
    }
    u32 p[8];
#pragma unroll
    for (int i = 0; i < 8; ++i) p[i] = pk2(acc[2 * i] * LOG2E, acc[2 * i + 1] * LOG2E);
    if (kind == 0) {
#pragma unroll
      for (int q = 0; q < 4; ++q)
        if (4 * rt + q < 5) { u32x2 v = {p[2 * q], p[2 * q + 1]}; *(u32x2*)(ws + WS_AP + (c * 5 + 4 * rt + q) * 1024 + t * 512 + r * 16 + hh * 8) = v; }
    } else if (32 * rt + r < NPB) {
      u32x4* o = (u32x4*)(ws + WS_BP + (c * NPB + 32 * rt + r) * 128 + (2 * t + hh) * 32);
      u32x4 v0 = {p[0], p[1], p[2], p[3]}, v1 = {p[4], p[5], p[6], p[7]};
      o[0] = v0; o[1] = v1;
    }
  }
  __syncthreads();
  ST(0, 2);
#pragma unroll
  for (int i = 0; i < 4; ++i) {
    const u32 p = ldsr<u32>(L + K1_HIST + 4 * bk[i]) + rank[i];
    const u32 dl = d4[i] - bk[i] * NPB;
    const u32 meta = s4[i] | (dl << 17) | ((dl >> 3) << 26);
    const u32 a = L + K1_REC + p * 12;
    const u32x4 e = i < 2 ? e0 : e1;
    ldsw<u32>(a, e[2 * (i & 1)]); ldsw<u32>(a + 4, e[2 * (i & 1) + 1]); ldsw<u32>(a + 8, meta);
  }
  __syncthreads();
  ST(0, 3);
  u32x4* ro = (u32x4*)(ws + WS_REC) + c * 1920;
#pragma unroll
  for (int j = 0; j < 3; ++j) ro[j * 640 + tid] = ldsr<u32x4>(L + K1_REC + (j * 640 + tid) * 16);
  ST(0, 4);
}

constexpr int Q_DESC = 0, Q_SRC = 51200, Q_DL = 57600, Q_END = 60800;
constexpr int L_CNT = 60800, L_POSD = 60864, L_AP = 61504, L_WEXT = 66624, L_HROW = 67648, L_R = 72768;
constexpr int SLOT_SZ = 8 * 272 + 256;
constexpr int R_DMA = L_R, R_IMG = L_R + 32768, IMG_SZ = 2304, R_SLOT = R_IMG + 8 * IMG_SZ, R_END = R_SLOT + 12 * SLOT_SZ;
constexpr int L_MASK = L_CNT + 32, L_WXF = L_R + 80896;
constexpr int K2_LDS = L_WXF + 256;
static_assert(R_END <= L_WXF, "lds");

DI void dma1(const char* base, u32 voff, u32 m0v) {
  u32 keep;
  asm volatile("s_mov_b32 %0, m0\n\ts_mov_b32 m0, %3\n\ts_nop 0\n\tglobal_load_lds_dwordx4 %1, %2\n\ts_mov_b32 m0, %0"
               : "=&s"(keep) : "v"(voff), "s"(base), "s"(m0v) : "memory");
}
DI void dma4(const char* base, u32 o0, u32 o1, u32 o2, u32 o3, u32 m0v) {
  u32 keep;
  asm volatile("s_mov_b32 %0, m0\n\ts_mov_b32 m0, %6\n\ts_nop 0\n\t"
               "global_load_lds_dwordx4 %1, %5\n\tglobal_load_lds_dwordx4 %2, %5 offset:1024\n\t"
               "global_load_lds_dwordx4 %3, %5 offset:2048\n\tglobal_load_lds_dwordx4 %4, %5 offset:3072\n\ts_mov_b32 m0, %0"
               : "=&s"(keep) : "v"(o0), "v"(o1), "v"(o2), "v"(o3), "s"(base), "s"(m0v) : "memory");
}

DI void stage_record(u32 L, u32x3 rec, u32x2 ps) {
  const u32 meta = rec[2];
  const f32x4 pd = ldsr<f32x4>(L + L_POSD + ((meta >> 16) & 0xFF) * 8);
  u32 p = __hip_atomic_fetch_add(LDSP(u32, L + L_CNT + (meta >> 24)), 1u, __ATOMIC_RELAXED, __HIP_MEMORY_SCOPE_WORKGROUP);
  const float dx = pd[0] - lo2f(ps[0]), dy = pd[1] - hi2f(ps[0]), dz = pd[2] - lo2f(ps[1]);
  const u32 w3 = pk2(dy, dz), t0 = pk2(0.f, dx);
  const float rx = hi2f(t0), ry = lo2f(w3), rz = hi2f(w3);
  const float d2 = rx * rx + ry * ry + rz * rz;
  const u32x4 desc = {rec[0], rec[1], pk2(LOG2E * d2, dx), w3};
  p = min(p, (u32)(5 * QCAP - 1));
  ldsw<u32x4>(L + Q_DESC + p * 16, desc);
  ldsw<u16>(L + Q_SRC + p * 2, (u16)meta);
  ldsw<u8>(L + Q_DL + p, (u8)(meta >> 16));
}

__global__ __launch_bounds__(512) void k2_main(const u16* __restrict__ h, const u16* __restrict__ pos, const u16* __restrict__ w_x,
                                               const char* __restrict__ ws, u16* __restrict__ out) {
  __shared__ __attribute__((aligned(16))) char smem[K2_LDS];
  const u32 L = (u32)(size_t)smem;
  const int b = (blockIdx.x & 7) * 32 + (blockIdx.x >> 3);
  if (b >= NBK) return;
  ST(1, 0);
  const int tid = threadIdx.x, lane = tid & 63, w = __builtin_amdgcn_readfirstlane(tid >> 6);
  const int r = lane & 31, hh = lane >> 5;
  const u32* tab = (const u32*)(ws + WS_TAB);
  const char* recs = ws + WS_REC;

  u32 te[8];
#pragma unroll
  for (int it = 0; it < 8; ++it) { const int c = it * 32 + (tid >> 4); te[it] = c < NCH ? tab[c * 256 + b] : 0u; }
  for (int k = w; k < 79; k += 8) dma1(ws + WS_P4, k * 1024 + lane * 16, L + L_R + k * 1024);
  if (w < 5) { dma1(ws + WS_AP, (b * 5 + w) * 1024 + lane * 16, L + L_AP + w * 1024);
               dma1((const char*)h, (b * 5 + w) * 1024 + lane * 16, L + L_HROW + w * 1024); }
  if (w == 5) dma1(ws + WS_WX, lane * 16, L + L_WEXT);
  float posown = 0.f;
  if (tid >= 384 && tid < 504) posown = bf2f(pos[b * 120 + tid - 384]);
  if (tid < NPB) { const u16* pp = pos + (b * NPB + tid) * 3; f32x4 v = {bf2f(pp[0]), bf2f(pp[1]), bf2f(pp[2]), 0.f}; ldsw<f32x4>(L + L_POSD + tid * 16, v); }
  if (w == 1) ldsw<float>(L + L_WXF + lane * 4, LN2 * bf2f(w_x[lane]));
  {
    const u32x4 z = {0, 0, 0, 0};
    for (int i = tid; i < Q_END / 16; i += 512) ldsw<u32x4>(L + i * 16, z);
    if (tid < 16) ldsw<u32>(L + L_CNT + tid * 4, tid < 5 ? tid * QCAP : 0);
  }
  u32x3 rec[8], rec2[8];
  bool any2[8];
  const u32 i16 = tid & 15;
  u32 big = 0;
#pragma unroll
  for (int it = 0; it < 8; ++it) {
    const u32 cnt = te[it] >> 16, st = te[it] & 0xFFFF;
    const char* p = recs + ((it * 32 + (tid >> 4)) * CH + st + i16) * 12;
    if (i16 < cnt) rec[it] = *(const u32x3*)p;
    any2[it] = __any(cnt > 16);
    if (any2[it]) { if (i16 + 16 < cnt) rec2[it] = *(const u32x3*)(p + 192); }
    big |= cnt > 32;
  }
  asm volatile("s_waitcnt vmcnt(0)" ::: "memory");
  __syncthreads();
  ST(1, 1);
  if (big) ldsw<u32>(L + L_CNT + 28, 1u);
#pragma unroll
  for (int it = 0; it < 8; ++it) {
    const u32 cnt = te[it] >> 16;
    if (i16 < cnt) stage_record(L, rec[it], ldsr<u32x2>(L + L_R + (rec[it][2] & 0xFFFF) * 8));
    if (any2[it]) { if (i16 + 16 < cnt) stage_record(L, rec2[it], ldsr<u32x2>(L + L_R + (rec2[it][2] & 0xFFFF) * 8)); }
  }
  __syncthreads();
  ST(1, 2);
  bool slow;
  {
    const u32 cv = ldsr<u32>(L + L_CNT + (lane & 7) * 4);
    slow = __any((lane & 7) < 5 ? cv > (u32)((lane & 7) + 1) * QCAP : cv != 0);
    const u32x4 z = {0, 0, 0, 0};
    for (int i = tid; i < ((slow ? R_END : R_SLOT) - R_IMG) / 16; i += 512) ldsw<u32x4>(L + R_IMG + i * 16, z);
  }
  __syncthreads();

  const u32 dmaB = L + R_DMA + w * 4096, imgB = L + R_IMG + w * IMG_SZ;
  u32 cq[4], fa[4];
#pragma unroll
  for (int q = 0; q < 4; ++q) {
    cq[q] = (((lane & 7) ^ (q | (((lane >> 4) & 1) << 2))) * 16) + 3072 - 1024 * q;
    fa[q] = dmaB + (r & 3) * 1024 + (r >> 2) * 128 + (((2 * q + hh) ^ ((r & 3) | (((r >> 3) & 1) << 2))) * 16);
  }
  const char* bpb = ws + WS_BP - 3072;
  const u32 tra = imgB + (2 * ((lane >> 4) & 1) + ((lane & 3) >> 1)) * 576 + (4 * hh + ((lane >> 2) & 3)) * 16 + 8 * (lane & 1);
  u32x4 idf[2];
#pragma unroll
  for (int s = 0; s < 2; ++s)
#pragma unroll
    for (int j = 0; j < 4; ++j) {
      const int c0 = 16 * hh + 8 * (j >> 1) + 4 * s + 2 * (j & 1);
      idf[s][j] = (r == c0 ? 0x3F80u : 0u) | (r == c0 + 1 ? 0x3F800000u : 0u);
    }

  int sc = 0, so = 0;
  for (;;) {
    if (slow) {
      __syncthreads();
      if (tid < 5) ldsw<u32>(L + L_CNT + tid * 4, tid * QCAP);
      __syncthreads();
      const u32 e = tab[sc * 256 + b];
      const int cnt = e >> 16, st = e & 0xFFFF;
      if (so + tid < cnt) {
        const u32x3 rc = *(const u32x3*)(recs + (sc * CH + st + so + tid) * 12);
        stage_record(L, rc, *(const u32x2*)(ws + WS_P4 + (rc[2] & 0xFFFF) * 8));
      }
      so += 512;
      if (so >= cnt) { so = 0; ++sc; }
      __syncthreads();
    }
    const u32 cv = ldsr<u32>(L + L_CNT + (lane & 7) * 4);
    int cn[5], P[6];
    P[0] = 0;
#pragma unroll
    for (int s = 0; s < 5; ++s) { cn[s] = __builtin_amdgcn_readlane(cv, s) - s * QCAP; P[s + 1] = P[s] + ((cn[s] + 31) >> 5); }
    const int T = P[5], tb = T >> 3, tr = T & 7;
    int lo = w * tb + min(w, tr), hi = lo + tb + (w < tr ? 1 : 0);
    if (slow) { lo = w == 0 ? P[0] : w == 1 ? P[1] : w == 2 ? P[2] : w == 3 ? P[3] : w == 4 ? P[4] : 0;
                hi = w == 0 ? P[1] : w == 1 ? P[2] : w == 2 ? P[3] : w == 3 ? P[4] : w == 4 ? P[5] : 0; }
    auto info = [&](int g, int& s, int& p0, int& rem) {
      s = (g >= P[1]) + (g >= P[2]) + (g >= P[3]) + (g >= P[4]);
      const int Ps = s == 0 ? P[0] : s == 1 ? P[1] : s == 2 ? P[2] : s == 3 ? P[3] : P[4];
      const int cs = s == 0 ? cn[0] : s == 1 ? cn[1] : s == 2 ? cn[2] : s == 3 ? cn[3] : cn[4];
      p0 = s * QCAP + 32 * (g - Ps); rem = cs - 32 * (g - Ps);
    };
    auto issue_dma = [&](int p0) {
      const u32x2 ix = ldsr<u32x2>(L + Q_SRC + p0 * 2 + (lane >> 3) * 8);
      dma4(bpb, (ix[0] & 0xFFFF) * 128 + cq[0], (ix[0] >> 16) * 128 + cq[1], (ix[1] & 0xFFFF) * 128 + cq[2], (ix[1] >> 16) * 128 + cq[3], dmaB);
    };
    f32x16 acc0, acc1;
    auto flush = [&](int s) {
      const u32 sa = L + R_SLOT + (w + s) * SLOT_SZ;
      float pv = 0.f;
#pragma unroll
      for (int q = 0; q < 4; ++q) {
        const f32x4 w0 = ldsr<f32x4>(L + L_WXF + q * 32 + hh * 16), w1 = ldsr<f32x4>(L + L_WXF + 128 + q * 32 + hh * 16);
#pragma unroll
        for (int i = 0; i < 4; ++i) pv += acc0[4 * q + i] * w0[i] + acc1[4 * q + i] * w1[i];
      }
      if (slow) pv += ldsr<float>(sa + 8 * 272 + lane * 4);
      ldsw<float>(sa + 8 * 272 + lane * 4, pv);
      if (r < 8) {
        const u32 ma = sa + r * 272 + hh * 16;
#pragma unroll
        for (int q = 0; q < 4; ++q) {
          f32x4 v0 = {acc0[4 * q], acc0[4 * q + 1], acc0[4 * q + 2], acc0[4 * q + 3]}, v1 = {acc1[4 * q], acc1[4 * q + 1], acc1[4 * q + 2], acc1[4 * q + 3]};
          if (slow) { v0 += ldsr<f32x4>(ma + q * 32); v1 += ldsr<f32x4>(ma + 128 + q * 32); }
          ldsw<f32x4>(ma + q * 32, v0); ldsw<f32x4>(ma + 128 + q * 32, v1);
        }
      }
      if (lane == 0) __hip_atomic_fetch_or(LDSP(u32, L + L_MASK + s * 4), 1u << w, __ATOMIC_RELAXED, __HIP_MEMORY_SCOPE_WORKGROUP);
    };
    if (lo < hi) {
      int s, p0, rem, cur = -1;
      info(lo, s, p0, rem);
      issue_dma(p0);
      u32x4 wf0, wf1;
      for (int g = lo; g < hi; ++g) {
        if (s != cur) {
          if (cur >= 0) flush(cur);
          cur = s;
#pragma unroll
          for (int i = 0; i < 16; ++i) { acc0[i] = 0.f; acc1[i] = 0.f; }
          const u32 a = L + (hh ? L_AP + s * 1024 : L_WEXT) + r * 16;
          wf0 = ldsr<u32x4>(a); wf1 = ldsr<u32x4>(a + 512);
        }
        const u32 da = L + Q_DESC + (p0 + r) * 16;
        const u32 ia = imgB + r * 16 - 16 * s + ldsr<u8>(L + Q_DL + p0 + r);
        const u32x2 dd = ldsr<u32x2>(da + 8);
        const bool act = lane < min(rem, 32);
        if (act) { ldsw<u16>(ia, 0x3F80); ldsw<u16>(ia + 576, dd[0] >> 16); ldsw<u16>(ia + 1152, dd[1]); ldsw<u16>(ia + 1728, dd[1] >> 16); }
        asm volatile("s_waitcnt vmcnt(0)" ::: "memory");
        const u32x4 f0 = ldsr<u32x4>(fa[0]), f1 = ldsr<u32x4>(fa[1]), f2 = ldsr<u32x4>(fa[2]), f3 = ldsr<u32x4>(fa[3]);
        const u32x4 ext = ldsr<u32x4>(hh ? imgB + r * 16 : da);
        const u32x2 t0 = trread(tra), t1 = trread(tra + 128), t2 = trread(tra + 256), t3 = trread(tra + 384);
        if (act) { ldsw<u16>(ia, 0); ldsw<u16>(ia + 576, 0); ldsw<u16>(ia + 1152, 0); ldsw<u16>(ia + 1728, 0); }
        int s2 = s, p2 = p0, rem2 = rem;
        asm volatile("s_waitcnt lgkmcnt(0)" ::: "memory");
        if (g + 1 < hi) { info(g + 1, s2, p2, rem2); issue_dma(p2); }
        f32x16 z;
#pragma unroll
        for (int i = 0; i < 16; ++i) z[i] = 0.f;
        f32x16 x0 = mfma32(ext, wf0, z), x1 = mfma32(ext, wf1, z);
        x0 = mfma32(f0, idf[0], x0); x1 = mfma32(f2, idf[0], x1);
        x0 = mfma32(f1, idf[1], x0); x1 = mfma32(f3, idf[1], x1);
        u32x4 m0[2], m1[2];
#pragma unroll
        for (int i = 0; i < 8; ++i) {
          const float a = x0[2 * i], c = x0[2 * i + 1];
          m0[i >> 2][i & 3] = pk2(a * __builtin_amdgcn_rcpf(1.f + __builtin_amdgcn_exp2f(-a)), c * __builtin_amdgcn_rcpf(1.f + __builtin_amdgcn_exp2f(-c)));
        }
        const u32x4 sa = {t0[0], t0[1], t1[0], t1[1]}, sb = {t2[0], t2[1], t3[0], t3[1]};
        acc0 = mfma32(m0[0], sa, acc0); acc0 = mfma32(m0[1], sb, acc0);
#pragma unroll
        for (int i = 0; i < 8; ++i) {
          const float a = x1[2 * i], c = x1[2 * i + 1];
          m1[i >> 2][i & 3] = pk2(a * __builtin_amdgcn_rcpf(1.f + __builtin_amdgcn_exp2f(-a)), c * __builtin_amdgcn_rcpf(1.f + __builtin_amdgcn_exp2f(-c)));
        }
        acc1 = mfma32(m1[0], sa, acc1); acc1 = mfma32(m1[1], sb, acc1);
        s = s2; p0 = p2; rem = rem2;
      }
      flush(cur);
    }
    if (!slow || sc >= NCH) break;
  }
  ST(1, 3);

  const int rt = w >> 1, cp = w & 1, l16 = lane & 15, g4 = lane >> 4;
  u32x4 wh[2][4];
  if (w < 6) {
#pragma unroll
    for (int u = 0; u < 2; ++u)
#pragma unroll
      for (int ks = 0; ks < 4; ++ks) wh[u][ks] = ((const u32x4*)(ws + WS_WH))[((cp * 2 + u) * 4 + ks) * 64 + lane];
  }
  __syncthreads();
  ST(1, 4);
  if (w < 6) {
    const int row = min(16 * rt + l16, NPB - 1);
    u32x4 af[4];
    af[0] = ldsr<u32x4>(L + L_HROW + row * 128 + g4 * 16);
    af[1] = ldsr<u32x4>(L + L_HROW + row * 128 + g4 * 16 + 64);
    {
      f32x4 a[4];
#pragma unroll
      for (int i = 0; i < 4; ++i) a[i] = f32x4{0.f, 0.f, 0.f, 0.f};
      u32 mk = ldsr<u32>(L + L_MASK + (row >> 3) * 4);
      const u32 sb = L + R_SLOT + (row >> 3) * SLOT_SZ + (row & 7) * 272 + g4 * 32;
      while (mk) {
        const u32 sa = sb + __builtin_ctz(mk) * SLOT_SZ;
        mk &= mk - 1;
        a[0] += ldsr<f32x4>(sa); a[1] += ldsr<f32x4>(sa + 16); a[2] += ldsr<f32x4>(sa + 128); a[3] += ldsr<f32x4>(sa + 144);
      }
#pragma unroll
      for (int i = 0; i < 4; ++i) { af[2 + (i >> 1)][2 * (i & 1)] = pk2(a[i][0] * LN2, a[i][1] * LN2); af[2 + (i >> 1)][2 * (i & 1) + 1] = pk2(a[i][2] * LN2, a[i][3] * LN2); }
    }
    f32x4 d0 = {0.f, 0.f, 0.f, 0.f}, d1 = d0;
#pragma unroll
    for (int ks = 0; ks < 4; ++ks) { d0 = mfma16(af[ks], wh[0][ks], d0); d1 = mfma16(af[ks], wh[1][ks], d1); }
#pragma unroll
    for (int i = 0; i < 4; ++i) {
      const int n = 16 * rt + 4 * g4 + i;
      if (n < NPB) {
        const u32 hv = ldsr<u32>(L + L_HROW + n * 128 + cp * 64 + l16 * 4);
        const float a = d0[i], c = d1[i];
        const float sa = a * __builtin_amdgcn_rcpf(1.f + __builtin_amdgcn_exp2f(-LOG2E * a));
        const float sb = c * __builtin_amdgcn_rcpf(1.f + __builtin_amdgcn_exp2f(-LOG2E * c));
        *(u32*)(out + (b * NPB + n) * 64 + cp * 32 + l16 * 2) = pk2(lo2f(hv) + sa, hi2f(hv) + sb);
      }
    }
  } else if (tid < 504) {
    const int o = tid - 384;
    const int n = o / 3, cc = o - 3 * n;
    u32 mk = ldsr<u32>(L + L_MASK + (n >> 3) * 4);
    const u32 sb = L + R_SLOT + (n >> 3) * SLOT_SZ + 8 * 272 + (8 * (cc + 1) + (n & 7)) * 4;
    float sum = 0.f;
    while (mk) { const u32 sa = sb + __builtin_ctz(mk) * SLOT_SZ; mk &= mk - 1; sum += ldsr<float>(sa) + ldsr<float>(sa + 128); }
    out[NN * 64 + b * 120 + o] = (u16)pk2(posown + sum, 0.f);
  }
  ST(1, 5);
}

extern "C" void kernel_launch(void* const* d_in, const int* in_sizes, int n_in, void* d_out, int out_size, void* d_ws,
                              size_t ws_size, hipStream_t stream) {
  const u16* h = (const u16*)d_in[0];
  const u16* pos = (const u16*)d_in[1];
  k1_bin<<<NCH + 2, 640, 0, stream>>>(h, pos, (const u32x4*)d_in[2], (const u32x4*)d_in[3], (const u32x4*)d_in[4],
                                      (const u16*)d_in[5], (const u16*)d_in[7], (char*)d_ws);
  k2_main<<<256, 512, 0, stream>>>(h, pos, (const u16*)d_in[6], (const char*)d_ws, (u16*)d_out);
}
```

```cpp
#include <hip/hip_runtime.h>
#include <hip/hip_bf16.h>
#include <stdint.h>


typedef unsigned short u16;
typedef unsigned u32;
typedef unsigned long long u64;
typedef u32 u32x2 __attribute__((ext_vector_type(2)));
typedef u32 u32x3 __attribute__((ext_vector_type(3)));
typedef u32 u32x4 __attribute__((ext_vector_type(4)));
typedef u32 u32x8 __attribute__((ext_vector_type(8)));
typedef u32 u32x16 __attribute__((ext_vector_type(16)));
typedef float f32x2 __attribute__((ext_vector_type(2)));
typedef float f32x4 __attribute__((ext_vector_type(4)));
typedef float f32x16 __attribute__((ext_vector_type(16)));
typedef __bf16 bf16x2 __attribute__((ext_vector_type(2)));
typedef __bf16 bf16x8 __attribute__((ext_vector_type(8)));
typedef short s16x4 __attribute__((ext_vector_type(4)));
#define DI __device__ __forceinline__
#define LDSP(T, a) ((__attribute__((address_space(3))) T*)(a))

constexpr int NN = 10000, EE = 640000;
constexpr int NPB = 40, NBK = 250;
constexpr int CH = 2560, NCH = 250;
constexpr int QCAP = 512;
constexpr float LOG2E = 1.4426950408889634f, LN2 = 0.6931471805599453f;

constexpr size_t WS_REC = 0;
constexpr size_t WS_ENT = WS_REC + (size_t)EE * 12;
constexpr size_t WS_Q = WS_ENT + 256 * 2048;
constexpr size_t WS_BP = WS_Q + 8 * 1024;
constexpr size_t WS_P4 = WS_BP + (size_t)NN * 128;

DI float bf2f(u16 x) { return __uint_as_float(((u32)x) << 16); }
DI u32 pk2(float a, float b) { f32x2 f = {a, b}; return __builtin_bit_cast(u32, __builtin_convertvector(f, bf16x2)); }
DI float lo2f(u32 x) { return __uint_as_float(x << 16); }
DI float hi2f(u32 x) { return __uint_as_float(x & 0xFFFF0000u); }
DI f32x16 mfma32(u32x4 a, u32x4 b, f32x16 c) {
  return __builtin_amdgcn_mfma_f32_32x32x16_bf16(__builtin_bit_cast(bf16x8, a), __builtin_bit_cast(bf16x8, b), c, 0, 0, 0); }
DI f32x4 mfma16(u32x4 a, u32x4 b, f32x4 c) {
  return __builtin_amdgcn_mfma_f32_16x16x32_bf16(__builtin_bit_cast(bf16x8, a), __builtin_bit_cast(bf16x8, b), c, 0, 0, 0); }
DI u32x2 trread(u32 a) { return __builtin_bit_cast(u32x2, __builtin_amdgcn_ds_read_tr16_b64_v4i16(LDSP(s16x4, a))); }
template <typename T> DI T ldsr(u32 a) { return *LDSP(T, a); }
template <typename T> DI void ldsw(u32 a, T v) { *LDSP(T, a) = v; }

DI void dma1_dev(const void* base, u32 voff, u32 m0v) {
  u32 keep;
  asm volatile("s_mov_b32 %0, m0\n\ts_mov_b32 m0, %3\n\ts_nop 0\n\tglobal_load_lds_dwordx4 %1, %2 sc1\n\ts_mov_b32 m0, %0"
               : "=&s"(keep) : "v"(voff), "s"(base), "s"(m0v) : "memory");
}
DI void bar() { asm volatile("s_waitcnt lgkmcnt(0)\n\ts_barrier" ::: "memory"); }
DI u32 lds_add(u32 a, u32 v) { return __hip_atomic_fetch_add(LDSP(u32, a), v, __ATOMIC_RELAXED, __HIP_MEMORY_SCOPE_WORKGROUP); }

constexpr int P1_HIST = 2176, L_PAY = 10368, P1_TR = 12416, P1_WE = 46720, P1_WH = 64128, L_ZERO = 64, L_CNT = 0, L_POSD = 256,
              L_WXF = 896, L_WEXT = 1152, R_SLOT = 2176, SLOT_SZ = 2432, L_BT = 2176, L_BUN = 31360, L_HROW = 36480, R_XB = 36480,
              R_XI = 44672, RP = 1056, DMA_SZ = 4224, R_DMA = 46720, R_IMG = 80512, IMGP = 1088, IMG_SZ = 4352, Q_DESC = 115712,
              QB = 7232, K_LDS = 163840;
DI float silu2(float y) { return y * __builtin_amdgcn_rcpf(1.f + __builtin_amdgcn_exp2f(y)); }

DI void slow_path(u32 L, int b, int tid, int w, const char* ws, const u16* pos, u32 fa, u32x4 idf0, u32x4 idf1, u32 tra) {
  const int lane = tid & 63, r = lane & 31, hh = lane >> 5;
  const u32 dmaB = L + R_DMA + w * DMA_SZ, imgB = L + R_IMG + w * IMG_SZ, sa = L + R_SLOT + 2 * w * SLOT_SZ, qm = L + R_SLOT;
  const auto rs = __builtin_amdgcn_make_buffer_rsrc((void*)(ws + WS_REC), 0, EE * 12, 0x00020000);
  __syncthreads();
  for (int i = tid; i < (K_LDS - R_IMG) / 16; i += 512) ldsw<u32x4>(L + R_IMG + i * 16, u32x4{0, 0, 0, 0});
  for (int i = tid; i < 5 * QCAP / 4; i += 512) ldsw<u32x4>(qm + i * 16, u32x4{0, 0, 0, 0});
  f32x16 acc0, acc1;
#pragma unroll
  for (int i = 0; i < 16; ++i) { acc0[i] = 0.f; acc1[i] = 0.f; }
  const u32 wfa = L + (hh ? L_BUN + min(w, 4) * 1024 : L_WEXT) + r * 16;
  const u32x4 wf0 = ldsr<u32x4>(wfa), wf1 = ldsr<u32x4>(wfa + 512);
  for (int sc = 0, so = 0; sc < NCH;) {
    __syncthreads();
    if (tid < 5) ldsw<u32>(L + L_CNT + tid * 4, 0);
    __syncthreads();
    const u64 e = __hip_atomic_load((const u64*)(ws + WS_ENT + sc * 2048 + b * 8), __ATOMIC_RELAXED, __HIP_MEMORY_SCOPE_AGENT);
    const u32 e0 = (u32)e, e1 = (u32)(e >> 32), st = e1 & 0xFFF;
    const int cnt = (e1 & 0x40000) ? e0 : (e0 & 63) + ((e0 >> 6) & 63) + ((e0 >> 16) & 63) + ((e0 >> 22) & 63) + ((e1 >> 12) & 63);
    if (so + tid < cnt) {
      const u32x3 rec = __builtin_amdgcn_raw_buffer_load_b96(rs, (sc * CH + st + so + tid) * 12, 0, 16);
      const u32 meta = rec[2];
      const u16* pp = pos + (meta & 0xFFFF) * 3;
      const f32x4 pd = ldsr<f32x4>(L + L_POSD + (meta >> 24) * 8);
      const u32 p = (meta >> 28) * QCAP + lds_add(L + L_CNT + (meta >> 28) * 4, 1u);
      const float dx = pd[0] - bf2f(pp[0]), dy = pd[1] - bf2f(pp[1]), dz = pd[2] - bf2f(pp[2]);
      const u32 w3 = pk2(dy, dz), t0 = pk2(0.f, dx);
      const float rx = hi2f(t0), ry = lo2f(w3), rz = hi2f(w3);
      ldsw<u32x4>(L + Q_DESC + p * 16, u32x4{rec[0], rec[1], pk2(-LOG2E * (rx * rx + ry * ry + rz * rz), dx), w3});
      ldsw<u32>(qm + p * 4, meta);
    }
    so += 512;
    if (so >= cnt) { so = 0; ++sc; }
    __syncthreads();
    const int n = w < 5 ? __builtin_amdgcn_readfirstlane(ldsr<u32>(L + L_CNT + min(w, 4) * 4)) : 0;
    for (int t0 = 0; t0 < n; t0 += 32) {
      const int p0 = w * QCAP + t0;
      const u32x4 ix = ldsr<u32x4>(qm + p0 * 4 + (lane >> 3) * 16);
#pragma unroll
      for (int q = 0; q < 4; ++q) dma1_dev(ws + WS_BP, (ix[q] & 0xFFFF) * 128 + (lane & 7) * 16, dmaB + q * RP);
      const u32 da = L + Q_DESC + (p0 + r) * 16;
      const u32 ia = imgB + r * 16 + ((ldsr<u32>(qm + (p0 + r) * 4) >> 24) & 14);
      const u32x2 dd = ldsr<u32x2>(da + 8);
      const bool act = lane < min(n - t0, 32);
      if (act) { ldsw<u16>(ia, 0x4000); ldsw<u16>(ia + IMGP, dd[0] >> 16); ldsw<u16>(ia + 2 * IMGP, dd[1]); ldsw<u16>(ia + 3 * IMGP, dd[1] >> 16); }
      asm volatile("s_waitcnt vmcnt(0)" ::: "memory");
      const u32x4 f0 = ldsr<u32x4>(fa), f1 = ldsr<u32x4>(fa + 32), f2 = ldsr<u32x4>(fa + 64), f3 = ldsr<u32x4>(fa + 96);
      const u32x4 ext = ldsr<u32x4>(hh ? imgB + r * 16 : da);
      const u32x2 t0_ = trread(tra), t1 = trread(tra + 128), t2 = trread(tra + 256), t3 = trread(tra + 384);
      if (act) { ldsw<u16>(ia, 0); ldsw<u16>(ia + IMGP, 0); ldsw<u16>(ia + 2 * IMGP, 0); ldsw<u16>(ia + 3 * IMGP, 0); }
      asm volatile("s_waitcnt lgkmcnt(0)" ::: "memory");
      f32x16 z;
#pragma unroll
      for (int i = 0; i < 16; ++i) z[i] = 0.f;
      const f32x16 x0 = mfma32(f1, idf1, mfma32(f0, idf0, mfma32(ext, wf0, z)));
      const f32x16 x1 = mfma32(f3, idf1, mfma32(f2, idf0, mfma32(ext, wf1, z)));
      u32x4 m0[2], m1[2];
#pragma unroll
      for (int i = 0; i < 8; ++i) { m0[i >> 2][i & 3] = pk2(silu2(x0[2 * i]), silu2(x0[2 * i + 1])); m1[i >> 2][i & 3] = pk2(silu2(x1[2 * i]), silu2(x1[2 * i + 1])); }
      const u32x4 s0 = {t0_[0], t0_[1], t1[0], t1[1]}, s1 = {t2[0], t2[1], t3[0], t3[1]};
      acc0 = mfma32(m0[1], s1, mfma32(m0[0], s0, acc0));
      acc1 = mfma32(m1[1], s1, mfma32(m1[0], s0, acc1));
    }
  }
  __syncthreads();
  if (w < 5) {
    float pv = 0.f;
#pragma unroll
    for (int q = 0; q < 4; ++q) {
      const f32x4 w0 = ldsr<f32x4>(L + L_WXF + q * 32 + hh * 16), w1 = ldsr<f32x4>(L + L_WXF + 128 + q * 32 + hh * 16);
#pragma unroll
      for (int i = 0; i < 4; ++i) pv += acc0[4 * q + i] * w0[i] + acc1[4 * q + i] * w1[i];
    }
    ldsw<float>(sa + 8 * 272 + lane * 4, pv);
    if (r < 8) {
#pragma unroll
      for (int q = 0; q < 4; ++q) {
        ldsw<f32x4>(sa + r * 272 + hh * 16 + q * 32, f32x4{acc0[4 * q], acc0[4 * q + 1], acc0[4 * q + 2], acc0[4 * q + 3]});
        ldsw<f32x4>(sa + r * 272 + hh * 16 + 128 + q * 32, f32x4{acc1[4 * q], acc1[4 * q + 1], acc1[4 * q + 2], acc1[4 * q + 3]});
      }
    }
  }
}

template <bool TR> DI void node_product(u32 L, int lane, int rt, int t, int krow0, float sc, u32 (&p)[8]) {
  const int r = lane & 31, hh = lane >> 5, row = min(32 * rt + r, NPB - 1);
  const u32 ha = L + L_HROW + row * 128;
  const u32 wa = L + P1_WE + (krow0 + 8 * hh + ((lane >> 2) & 3)) * 128 + (32 * t + 16 * ((lane >> 4) & 1) + 4 * (lane & 3)) * 2;
  f32x16 acc;
#pragma unroll
  for (int i = 0; i < 16; ++i) acc[i] = 0.f;
#pragma unroll
  for (int s = 0; s < 4; ++s) {
    const u32x4 hf = ldsr<u32x4>(ha + (((2 * s + hh) ^ (row & 7)) << 4));
    const u32x2 w0 = trread(wa + s * 2048), w1 = trread(wa + s * 2048 + 512);
    const u32x4 wf = {w0[0], w0[1], w1[0], w1[1]};
    acc = TR ? mfma32(wf, hf, acc) : mfma32(hf, wf, acc);
  }
#pragma unroll
  for (int i = 0; i < 8; ++i) p[i] = pk2(acc[2 * i] * sc, acc[2 * i + 1] * sc);
}

__global__ __launch_bounds__(512) void egnn_fused(const u16* __restrict__ h, const u16* __restrict__ pos, const char* __restrict__ ea,
                                                  const char* __restrict__ src, const char* __restrict__ dst, const u16* __restrict__ w_e,
                                                  const u16* __restrict__ w_x, const u16* __restrict__ w_h, char* __restrict__ ws,
                                                  u16* __restrict__ out) {
  __shared__ __attribute__((aligned(16))) char smem[K_LDS];
  u32 cpf;
  u64 pc0;
  asm volatile("s_getpc_b64 %0" : "=s"(pc0));
  asm volatile("global_load_dword %0, %1, %2" : "=&{v119}"(cpf) : "v"(min(threadIdx.x, 250u) * 64), "s"(pc0) : "memory");
  __builtin_amdgcn_sched_barrier(0);
  const u32 L = (u32)(size_t)smem;
  const int xg = blockIdx.x & 7, b = xg * 31 + min(xg, 2) + (blockIdx.x >> 3);
  const int tid = threadIdx.x, lane = tid & 63, w = __builtin_amdgcn_readfirstlane(tid >> 6);
  const int r = lane & 31, hh = lane >> 5;

  u32x4 d4, s4, e0, e1;
  u32x2 e2, oe;
  u32 d1, s1, oq, px, py, pz, nonce;
  asm volatile("global_load_dwordx4 %0, %2, %3\n\tglobal_load_dword %1, %4, %5"
               : "=&{v[64:67]}"(d4), "=&{v68}"(d1) : "v"(tid * 16), "s"(dst + b * (CH * 4)), "v"(tid * 4 + 8192), "s"(dst + b * (CH * 4)) : "memory");
  __builtin_amdgcn_sched_barrier(0);
  char* epub = ws + WS_ENT + b * 2048 + w * 256;
  const char* einc = ws + WS_ENT + w * (32 * 2048) + b * 8;
  const char* qrow = ws + WS_Q + (b >> 5) * 1024 + w * 128;
  char* qown = ws + WS_Q + w * 1024 + b * 4;
  asm volatile("global_load_dwordx2 %0, %2, %3 sc1\n\tglobal_load_dword %1, %4, %5 sc1" : "=&{v[116:117]}"(oe), "=&{v118}"(oq) : "v"(r * 2048), "s"(einc), "v"(0), "s"(qown) : "memory");
  ldsw<u32x4>(L + P1_HIST + 16 * tid, u32x4{0, 0, 0, 0});
  bar();
  asm volatile("global_load_dwordx4 %0, %5, %6\n\tglobal_load_dword %1, %7, %8\n\t"
               "global_load_dwordx4 %2, %9, %10\n\tglobal_load_dwordx4 %3, %9, %10 offset:16\n\tglobal_load_dwordx2 %4, %11, %12"
               : "=&{v[70:73]}"(s4), "=&{v74}"(s1), "=&{v[76:79]}"(e0), "=&{v[82:85]}"(e1), "=&{v[88:89]}"(e2)
               : "v"(tid * 16), "s"(src + b * (CH * 4)), "v"(tid * 4 + 8192), "s"(src + b * (CH * 4)), "v"(tid * 32), "s"(ea + b * (CH * 8)),
                 "v"(tid * 8 + 16384), "s"(ea + b * (CH * 8))
               : "memory");
  if (tid == 320) ldsw<u32>(L + P1_HIST + 5120, QB);
  if (tid >= 496) ldsw<u32x4>(L + L_CNT + (tid - 496) * 16, u32x4{0, 0, 0, 0});
  u32x16 sp;
  {
    const void* pp[6] = {w_e + 4096 + w * 512, w < 5 ? h + b * (NPB * 64) + w * 512 : w_e + 8192, w == 3 ? w_x : pos + b * (NPB * 3),
                         w_e + w * 512, w_h + w * 1024, qown};
#pragma unroll
    for (int i = 0; i < 6; ++i) { sp[2 * i] = (u32)(size_t)pp[i]; sp[2 * i + 1] = (u32)((size_t)pp[i] >> 32); }
    const size_t a = (size_t)(ws + WS_REC + b * (CH * 12));
    sp[12] = (u32)a; sp[13] = (u32)(a >> 32) & 0xFFFF; sp[14] = CH * 12; sp[15] = 0x00020000;
  }
  const u32x4 sm = {L + P1_WE + 8192 + w * 1024, w < 5 ? L + L_HROW + w * 1024 : L + P1_WE + 16384, L + P1_WE + w * 1024, L + P1_WH + w * 2048};
  const u32x4 pc = {L + P1_HIST + tid * 20, (u32)lane * 16, (u32)(w < 5 ? (lane >> 3) * 128 + (((lane & 7) ^ (lane >> 3)) << 4) : min(lane, 39) * 16),
                    (u32)(w == 3 ? min(lane, 61) * 2 : min(lane, NPB - 1) * 6)};

  asm volatile(
      "s_mov_b32 s41, m0\n"
      "v_mov_b32 v130, 1\n"
      "s_mov_b32 m0, s80\n"
      "s_mov_b32 s42, 52429\n"
      "global_load_lds_dwordx4 v145, s[64:65]\n"
      "s_mov_b32 m0, s81\n"
      "s_movk_i32 s43, -40\n"
      "global_load_lds_dwordx4 v146, s[66:67]\n"
      "s_waitcnt vmcnt(9)\n"
      "v_lshrrev_b32 v100, 1, v64\n"
      "v_and_b32 v100, 0x7ffc, v100\n"
      "ds_add_rtn_u32 v105, v100, v130 offset:2176\n"
      "v_lshrrev_b32 v101, 1, v65\n"
      "v_and_b32 v101, 0x7ffc, v101\n"
      "ds_add_rtn_u32 v106, v101, v130 offset:2176\n"
      "v_lshrrev_b32 v102, 1, v66\n"
      "v_and_b32 v102, 0x7ffc, v102\n"
      "ds_add_rtn_u32 v107, v102, v130 offset:2176\n"
      "v_lshrrev_b32 v103, 1, v67\n"
      "v_and_b32 v103, 0x7ffc, v103\n"
      "ds_add_rtn_u32 v108, v103, v130 offset:2176\n"
      "v_lshrrev_b32 v104, 1, v68\n"
      "v_and_b32 v104, 0x7ffc, v104\n"
      "ds_add_rtn_u32 v109, v104, v130 offset:2176\n"
      "v_mul_u32_u24 v110, s42, v64\n"
      "v_mul_u32_u24 v111, s42, v65\n"
      "v_mul_u32_u24 v112, s42, v66\n"
      "v_mul_u32_u24 v113, s42, v67\n"
      "v_mul_u32_u24 v114, s42, v68\n"
      "v_lshrrev_b32 v110, 21, v110\n"
      "v_lshrrev_b32 v111, 21, v111\n"
      "v_lshrrev_b32 v112, 21, v112\n"
      "v_lshrrev_b32 v113, 21, v113\n"
      "v_lshrrev_b32 v114, 21, v114\n"
      "v_mad_i32_i24 v110, v110, s43, v64\n"
      "v_mad_i32_i24 v111, v111, s43, v65\n"
      "v_mad_i32_i24 v112, v112, s43, v66\n"
      "v_mad_i32_i24 v113, v113, s43, v67\n"
      "v_mad_i32_i24 v114, v114, s43, v68\n"
      "v_lshlrev_b32 v110, 25, v110\n"
      "v_lshlrev_b32 v111, 25, v111\n"
      "v_lshlrev_b32 v112, 25, v112\n"
      "v_lshlrev_b32 v113, 25, v113\n"
      "v_lshlrev_b32 v114, 25, v114\n"
      "s_cmp_gt_u32 %[w], 3\n"
      "s_waitcnt lgkmcnt(0)\n"
      "s_barrier\n"
      "s_cbranch_scc1 L_NS_%=\n"
      "ds_read2_b32 v[124:125], v144 offset1:1\n"
      "ds_read2_b32 v[126:127], v144 offset0:2 offset1:3\n"
      "ds_read_b32 v128, v144 offset:16\n"
      "v_mov_b32 v139, 7296\n"
      "s_mov_b32 s42, -7232\n"
      "v_mov_b32 v140, 0x40000\n"
      "s_waitcnt lgkmcnt(0)\n"
      "v_add3_u32 v131, v124, v125, v126\n"
      "v_add3_u32 v131, v131, v127, v128\n"
      "v_mov_b32 v132, v131\n"
      "v_lshl_or_b32 v134, v126, 6, v124\n"
      "v_add_u32 v136, v124, v125\n"
      "v_add_u32_dpp v132, v132, v132 row_shr:1 row_mask:0xf bank_mask:0xf\n"
      "v_lshl_or_b32 v134, v125, 16, v134\n"
      "v_add_u32 v137, v136, v126\n"
      "v_add_u32_dpp v132, v132, v132 row_shr:2 row_mask:0xf bank_mask:0xf\n"
      "v_lshl_or_b32 v134, v127, 22, v134\n"
      "v_add_u32 v138, v137, v127\n"
      "v_add_u32_dpp v132, v132, v132 row_shr:4 row_mask:0xf bank_mask:0xf\n"
      "v_cmp_lt_u32 vcc, 32, v131\n"
      "v_lshlrev_b32 v135, 12, v128\n"
      "v_add_u32_dpp v132, v132, v132 row_shr:8 row_mask:0xf bank_mask:0xf\n"
      "v_cndmask_b32 v134, v134, v131, vcc\n"
      "v_cndmask_b32 v135, v135, v140, vcc\n"
      "v_add_u32_dpp v132, v132, v132 row_bcast:15 row_mask:0xa bank_mask:0xf\n"
      "v_lshlrev_b32 v140, 3, %[tid]\n"
      "s_nop 0\n"
      "v_add_u32_dpp v132, v132, v132 row_bcast:31 row_mask:0xc bank_mask:0xf\n"
      "s_mov_b32 exec_lo, 0\n"
      "s_mov_b32 exec_hi, 0x80000000\n"
      "ds_add_rtn_u32 v139, v139, v132\n"
      "s_mov_b64 exec, -1\n"
      "v_sub_u32 v131, v132, v131\n"
      "s_waitcnt lgkmcnt(0)\n"
      "v_readlane_b32 s43, v139, 63\n"
      "s_nop 1\n"
      "v_add_u32 v131, s43, v131\n"
      "v_add_u32 v124, v124, v131\n"
      "v_add_u32 v136, v136, v131\n"
      "v_add_u32 v137, v137, v131\n"
      "v_add_u32 v138, v138, v131\n"
      "ds_write2_b32 v144, v131, v124 offset1:1\n"
      "ds_write2_b32 v144, v136, v137 offset0:2 offset1:3\n"
      "ds_write_b32 v144, v138 offset:16\n"
      "v_add3_u32 v135, v135, v131, s42\n"
      "ds_write_b64 v140, v[134:135] offset:10368\n"
      "L_NS_%=:\n"
      "global_load_ushort v120, v147, s[68:69]\n"
      "global_load_ushort v121, v147, s[68:69] offset:2\n"
      "global_load_ushort v122, v147, s[68:69] offset:4\n"
      "s_mov_b32 m0, s82\n"
      "s_nop 0\n"
      "global_load_lds_dwordx4 v145, s[70:71]\n"
      "s_mov_b32 m0, s83\n"
      "s_nop 0\n"
      "global_load_lds_dwordx4 v145, s[72:73]\n"
      "global_load_lds_dwordx4 v145, s[72:73] offset:1024\n"
      "v_mov_b32_sdwa v110, v105 dst_sel:BYTE_2 dst_unused:UNUSED_PRESERVE src0_sel:BYTE_0\n"
      "v_mov_b32_sdwa v111, v106 dst_sel:BYTE_2 dst_unused:UNUSED_PRESERVE src0_sel:BYTE_0\n"
      "v_mov_b32_sdwa v112, v107 dst_sel:BYTE_2 dst_unused:UNUSED_PRESERVE src0_sel:BYTE_0\n"
      "v_mov_b32_sdwa v113, v108 dst_sel:BYTE_2 dst_unused:UNUSED_PRESERVE src0_sel:BYTE_0\n"
      "v_mov_b32_sdwa v114, v109 dst_sel:BYTE_2 dst_unused:UNUSED_PRESERVE src0_sel:BYTE_0\n"
      "s_waitcnt vmcnt(13)\n"
      "v_readfirstlane_b32 %[nonce], v118\n"
      "v_lshrrev_b32 v116, 19, v117\n"
      "v_mov_b32 v117, 0\n"
      "L_NC_%=:\n"
      "s_add_u32 %[nonce], %[nonce], 1\n"
      "s_and_b32 %[nonce], %[nonce], 0x1fff\n"
      "v_cmp_eq_u32 vcc, %[nonce], v116\n"
      "s_cbranch_vccnz L_NC_%=\n"
      "v_mov_b32 v118, %[nonce]\n"
      "s_mov_b64 exec, 1\n"
      "global_store_dword v117, v118, s[74:75] sc1\n"
      "s_mov_b64 exec, -1\n"
      "s_waitcnt vmcnt(9)\n"
      "v_mov_b64 v[92:93], v[76:77]\n"
      "v_mov_b64 v[96:97], v[82:83]\n"
      "v_or_b32 v94, v70, v110\n"
      "v_or_b32 v80, v71, v111\n"
      "v_or_b32 v98, v72, v112\n"
      "v_or_b32 v86, v73, v113\n"
      "v_or_b32 v90, v74, v114\n"
      "s_waitcnt lgkmcnt(0)\n"
      "s_barrier\n"
      "ds_read_b32 v100, v100 offset:2176\n"
      "ds_read_b32 v101, v101 offset:2176\n"
      "ds_read_b32 v102, v102 offset:2176\n"
      "ds_read_b32 v103, v103 offset:2176\n"
      "ds_read_b32 v104, v104 offset:2176\n"
      "s_waitcnt lgkmcnt(4)\n"
      "v_add_lshl_u32 v100, v100, v105, 4\n"
      "ds_write_b96 v100, v[92:94]\n"
      "s_waitcnt lgkmcnt(4)\n"
      "v_add_lshl_u32 v101, v101, v106, 4\n"
      "ds_write_b96 v101, v[78:80]\n"
      "s_waitcnt lgkmcnt(4)\n"
      "v_add_lshl_u32 v102, v102, v107, 4\n"
      "ds_write_b96 v102, v[96:98]\n"
      "s_waitcnt lgkmcnt(4)\n"
      "v_add_lshl_u32 v103, v103, v108, 4\n"
      "ds_write_b96 v103, v[84:86]\n"
      "s_waitcnt lgkmcnt(4)\n"
      "v_add_lshl_u32 v104, v104, v109, 4\n"
      "ds_write_b96 v104, v[88:90]\n"
      "v_lshlrev_b32 v124, 4, %[tid]\n"
      "v_add_u32 v124, 115712, v124\n"
      "v_mul_u32_u24 v125, 12, %[tid]\n"
      "s_cmp_gt_u32 %[w], 3\n"
      "s_waitcnt vmcnt(7) lgkmcnt(0)\n"
      "s_barrier\n"
      "s_cbranch_scc1 L_NO_%=\n"
      "ds_read_b96 v[64:66], v124\n"
      "ds_read_b96 v[68:70], v124 offset:4096\n"
      "ds_read_b96 v[72:74], v124 offset:8192\n"
      "ds_read_b96 v[76:78], v124 offset:12288\n"
      "ds_read_b96 v[80:82], v124 offset:16384\n"
      "ds_read_b96 v[84:86], v124 offset:20480\n"
      "ds_read_b96 v[88:90], v124 offset:24576\n"
      "ds_read_b96 v[92:94], v124 offset:28672\n"
      "ds_read_b96 v[96:98], v124 offset:32768\n"
      "ds_read_b96 v[100:102], v124 offset:36864\n"
      "s_waitcnt lgkmcnt(9)\n"
      "buffer_store_dwordx3 v[64:66], v125, s[76:79], 0 offen sc1\n"
      "s_waitcnt lgkmcnt(8)\n"
      "buffer_store_dwordx3 v[68:70], v125, s[76:79], 0 offen offset:3072 sc1\n"
      "s_movk_i32 s42, 6144\n"
      "s_waitcnt lgkmcnt(7)\n"
      "buffer_store_dwordx3 v[72:74], v125, s[76:79], s42 offen sc1\n"
      "s_waitcnt lgkmcnt(6)\n"
      "buffer_store_dwordx3 v[76:78], v125, s[76:79], s42 offen offset:3072 sc1\n"
      "s_movk_i32 s42, 12288\n"
      "s_waitcnt lgkmcnt(5)\n"
      "buffer_store_dwordx3 v[80:82], v125, s[76:79], s42 offen sc1\n"
      "s_waitcnt lgkmcnt(4)\n"
      "buffer_store_dwordx3 v[84:86], v125, s[76:79], s42 offen offset:3072 sc1\n"
      "s_movk_i32 s42, 18432\n"
      "s_waitcnt lgkmcnt(3)\n"
      "buffer_store_dwordx3 v[88:90], v125, s[76:79], s42 offen sc1\n"
      "s_waitcnt lgkmcnt(2)\n"
      "buffer_store_dwordx3 v[92:94], v125, s[76:79], s42 offen offset:3072 sc1\n"
      "s_movk_i32 s42, 24576\n"
      "s_waitcnt lgkmcnt(1)\n"
      "buffer_store_dwordx3 v[96:98], v125, s[76:79], s42 offen sc1\n"
      "s_waitcnt lgkmcnt(0)\n"
      "buffer_store_dwordx3 v[100:102], v125, s[76:79], s42 offen offset:3072 sc1\n"
      "s_waitcnt vmcnt(14)\n"
      "s_branch L_NE_%=\n"
      "L_NO_%=:\n"
      "s_waitcnt vmcnt(4)\n"
      "L_NE_%=:\n"
      "s_mov_b32 m0, s41\n"
      : "+{v[64:67]}"(d4), "+{v68}"(d1), "+{v[70:73]}"(s4), "+{v74}"(s1), "+{v[76:79]}"(e0), "+{v[82:85]}"(e1), "+{v[88:89]}"(e2), "+{v[116:117]}"(oe), "+{v118}"(oq), "+{v119}"(cpf),
        "={v120}"(px), "={v121}"(py), "={v122}"(pz), [nonce] "=&s"(nonce)
      : "{v[144:147]}"(pc), "{s[64:79]}"(sp), "{s[80:83]}"(sm), [w] "s"(w), [tid] "v"(tid)
      : "v69", "v75", "v80", "v81", "v86", "v87", "v90", "v91", "v92", "v93", "v94", "v95", "v96", "v97", "v98", "v99",
        "v100", "v101", "v102", "v103", "v104", "v105", "v106", "v107", "v108", "v109", "v110", "v111", "v112", "v113", "v114", "v115",
        "v123", "v124", "v125", "v126", "v127", "v128", "v129", "v130", "v131", "v132", "v133", "v134", "v135", "v136", "v137", "v138",
        "v139", "v140", "v141", "v142", "v143", "s41", "s42", "s43", "s44", "s45", "s46", "s47", "s48", "s49", "s50", "vcc",
        "scc", "memory");
  if (w >= 4) {
    u32 p[8];
    node_product<true>(L, lane, (w >> 1) & 1, w & 1, 64, -LOG2E, p);
    const auto rs = __builtin_amdgcn_make_buffer_rsrc((void*)(ws + WS_BP + b * (NPB * 128)), 0, NPB * 128, 0x00020000);
    const u32 ta = L + P1_TR + (w - 4) * 2560;
    ldsw<u32x4>(ta + r * 80 + hh * 32, u32x4{p[0], p[1], p[2], p[3]});
    ldsw<u32x4>(ta + r * 80 + hh * 32 + 16, u32x4{p[4], p[5], p[6], p[7]});
    const u32 o = (32 * ((w >> 1) & 1) + (lane >> 2)) * 128 + (w & 1) * 64 + (lane & 3) * 16;
    __builtin_amdgcn_raw_buffer_store_b128(ldsr<u32x4>(ta + (lane >> 2) * 80 + (lane & 3) * 16), rs, o, 0, 16);
    __builtin_amdgcn_raw_buffer_store_b128(ldsr<u32x4>(ta + (lane >> 2) * 80 + (lane & 3) * 16 + 1280), rs, o + 2048, 0, 16);
    if (w == 6) {
      const auto rp = __builtin_amdgcn_make_buffer_rsrc((void*)(ws + WS_P4 + b * (NPB * 16)), 0, NPB * 16, 0x00020000);
      __builtin_amdgcn_raw_buffer_store_b128(u32x4{px << 16, py << 16, pz << 16, 0}, rp, lane * 16, 0, 16);
      if (lane < NPB) ldsw<u32x4>(L + L_POSD + lane * 16, u32x4{px << 16, py << 16, pz << 16, 0});
    }
  }
  u32 qv;
  asm volatile("global_load_dword %0, %1, %2 sc1" : "=&v"(qv) : "v"(r * 4), "s"(qrow) : "memory");
  const u32 dmaB = L + R_DMA + w * DMA_SZ, imgB = L + R_IMG + w * IMG_SZ, slotB = L + R_SLOT + w * SLOT_SZ, xb = L + R_XB + w * 1024, xi = L + R_XI + w * 256;
  const char* p4b = ws + WS_P4;
  const size_t ra = (size_t)(ws + WS_REC + w * (32 * CH * 12)), ba = (size_t)(ws + WS_BP - 1024 - 3 * RP);
  const u32x4 recS = {(u32)ra, (u32)(ra >> 32) & 0xFFFF, 0x01000000, 0x00020000};
  const u32x4 bpS = {(u32)ba, ((u32)(ba >> 32) & 0xFFFF) | (16u << 16), 0x10000000, 0x00800000};
  u32x4 idf0, idf1, k0, k1, k2, k3, k4;
#pragma unroll
  for (int q = 0; q < 4; ++q) {
    const u32 dz = r - (16 * hh + 8 * (q >> 1) + 2 * (q & 1));
    idf0[q] = dz < 2 ? 0x3F80u << (16 * dz) : 0u;
    idf1[q] = dz - 4 < 2 ? 0x3F80u << (16 * (dz - 4)) : 0u;
  }
  k0[0] = imgB + (2 * ((lane >> 4) & 1) + ((lane & 3) >> 1)) * IMGP + (4 * hh + ((lane >> 2) & 3)) * 16 + 8 * (lane & 1);
  k0[1] = (hh ? imgB : xb) + r * 16;
  k0[2] = hh ? 0u : ~0u;
  k0[3] = dmaB + (r & 3) * RP + (r >> 2) * 128 + hh * 16;
  k1[0] = xi + (lane >> 3) * 16;
  k1[1] = imgB + lane * 16;
  k1[2] = 0u;
  k1[3] = 0u;
  k2[0] = xb + lane * 16;
  k2[1] = L + (hh ? L_BUN : L_WEXT) + r * 16;
  k2[2] = L + L_WXF + hh * 16;
  k2[3] = 8 * 272 + lane * 4;
  k3[0] = r * 272 + hh * 16;
  k3[1] = lane;
  k3[2] = xi + lane * 4;
  k3[3] = 1024 - 128 * (r >> 2);
  k4[0] = r * 4;
  k4[1] = ((lane >> 4) * CH + (lane & 15)) * 12;
  k4[2] = L + L_BT + w * 512 + (lane >> 4) * 16;
  k4[3] = lane & 15;
  asm volatile("" : "+v"(k0), "+v"(k1), "+v"(k2), "+v"(k3), "+v"(k4), "+v"(idf0), "+v"(idf1));
  u32x4 pay;
  {
    const u32x2 p2 = ldsr<u32x2>(L + L_PAY + w * 256 + r * 8);
    asm volatile("s_waitcnt vmcnt(0)" : "+v"(qv) :: "memory");
    pay = u32x4{p2[0], p2[1], 0, qv & 0x1FFF};
    bar();
    if (lane < (w == 7 ? 26 : 32))
      __hip_atomic_store((u64*)(epub + lane * 8), ((u64)(pay[1] | (pay[3] << 19)) << 32) | pay[0], __ATOMIC_RELAXED, __HIP_MEMORY_SCOPE_AGENT);
    asm volatile("" ::: "memory");
  }
  if (w == 3) ldsw<float>(L + L_WXF + lane * 4, -LN2 * bf2f(lane == 62 ? py : lane == 63 ? pz : px));
  if (w == 2) {
    const u32 a = L + P1_WE + 16384 + lane * 2;
    ldsw<u32x4>(L + L_WEXT + lane * 16, u32x4{pk2(-LOG2E * bf2f(ldsr<u16>(a + 128)), -LOG2E * bf2f(ldsr<u16>(a + 256))),
                                              pk2(-LOG2E * bf2f(ldsr<u16>(a + 384)), -LOG2E * bf2f(ldsr<u16>(a + 512))), (u32)ldsr<u16>(a), 0});
  }
  if (w >= 4) {
    u32 p[8];
    const int rt = (w >> 1) & 1;
    node_product<false>(L, lane, rt, w & 1, 0, -0.5f * LOG2E, p);
#pragma unroll
    for (int q = 0; q < 4; ++q)
      if (4 * rt + q < 5) ldsw<u32x2>(L + L_BUN + (4 * rt + q) * 1024 + (w & 1) * 512 + r * 16 + hh * 8, u32x2{p[2 * q], p[2 * q + 1]});
  }
  f32x4 d0 = {0.f, 0.f, 0.f, 0.f}, d1v = d0;
  u32x16 wm;
  u32x8 hu;
  u32x4 k5;
  {
    const int rt = min(w >> 1, 2), cp = w & 1, l16 = lane & 15, g4 = lane >> 4, nrow = min(16 * rt + l16, NPB - 1);
    const u32 wa = L + P1_WH + (8 * g4 + ((lane >> 2) & 3)) * 128 + (32 * cp + 4 * (lane & 3)) * 2;
#pragma unroll
    for (int ks = 0; ks < 4; ++ks) {
      const u32x2 a0 = trread(wa + ks * 4096), a1 = trread(wa + ks * 4096 + 512), b0 = trread(wa + ks * 4096 + 32), b1 = trread(wa + ks * 4096 + 544);
      if (ks < 2) {
        const u32x4 hf = ldsr<u32x4>(L + L_HROW + nrow * 128 + (((4 * ks + g4) ^ (nrow & 7)) << 4));
        d0 = mfma16(u32x4{a0[0], a0[1], a1[0], a1[1]}, hf, d0); d1v = mfma16(u32x4{b0[0], b0[1], b1[0], b1[1]}, hf, d1v);
      } else {
        const int o = 8 * (ks - 2);
        wm[o] = a0[0]; wm[o + 1] = a0[1]; wm[o + 2] = a1[0]; wm[o + 3] = a1[1]; wm[o + 4] = b0[0]; wm[o + 5] = b0[1]; wm[o + 6] = b1[0]; wm[o + 7] = b1[1];
      }
    }
    const u32 ha = L + L_HROW + nrow * 128 + (g4 & 1) * 8;
    const u32x2 h0 = ldsr<u32x2>(ha + (((4 * cp + (g4 >> 1)) ^ (nrow & 7)) << 4)), h1 = ldsr<u32x2>(ha + (((4 * cp + 2 + (g4 >> 1)) ^ (nrow & 7)) << 4));
    hu = u32x8{h0[0] << 16, h0[0] & 0xFFFF0000u, h0[1] << 16, h0[1] & 0xFFFF0000u, h1[0] << 16, h1[0] & 0xFFFF0000u, h1[1] << 16, h1[1] & 0xFFFF0000u};
    if (w < 6) {
      k5 = u32x4{(u32)nrow & 56, L + R_SLOT + (nrow >> 3) * SLOT_SZ + (nrow & 7) * 272 + g4 * 32, (u32)((b * NPB + 16 * rt + (lane >> 2)) * 128 + cp * 64 + (lane & 3) * 16),
                 (L + L_BUN + w * 1280 + l16 * 80 + g4 * 8) | ((L + L_BUN + w * 1280 + (lane >> 2) * 80 + (lane & 3) * 16) << 16)};
    } else {
      const u32 o = min(tid - 384, 119), n = o / 3, cc = o - 3 * n;
      k5 = u32x4{n & 56, L + R_SLOT + (n >> 3) * SLOT_SZ + 8 * 272 + (8 * (cc + 1) + (n & 7)) * 4, (NN * 64 + b * 120 + o) * 2, 0};
      d0[0] = ldsr<float>(L + L_POSD + n * 16 + cc * 4);
    }
  }
  int slow;
  u32x4 sa;
  asm volatile(
      "s_mov_b32 s48, m0\n"
      "s_movk_i32 s49, 0x80\n"
      "s_mov_b32 %[slow], 0\n"
      "v_lshlrev_b32 v171, 1, v208\n"
      "v_lshlrev_b32 v170, 9, v208\n"
      "v_mov_b32 v168, v164\n"
      "s_mov_b32 s54, -1\n"
      "s_cmp_eq_u32 %[w], 7\n"
      "s_cselect_b32 s54, 0x3ffffff, s54\n"
      "s_mov_b32 s55, 0\n"
      "v_mov_b64 v[40:41], 0\n"
      "s_mov_b32 s40, 0x200000\n"
      "s_mov_b64 s[56:57], s[54:55]\n"
      "s_mov_b64 exec, s[54:55]\n"
      "global_load_dword v166, v208, %[qrow] sc1\n"
      "s_mov_b64 exec, s[56:57]\n"
      "global_load_dwordx2 v[40:41], v170, %[einc] sc1\n"
      "s_mov_b64 exec, -1\n"
      "s_mov_b32 s88, 0\n"
      "s_mov_b32 s89, 122880\n"
      "s_mov_b32 s90, 245760\n"
      "s_mov_b32 s91, 368640\n"
      "s_mov_b32 s92, 491520\n"
      "s_mov_b32 s93, 614400\n"
      "s_mov_b32 s94, 737280\n"
      "s_mov_b32 s95, 860160\n"
      "v_mov_b32 v130, 0xff0000\n"
      "s_mov_b32 s53, 0xbfb8aa3b\n"
      "v_and_b32 v181, 12, v208\n"
      "v_lshrrev_b32 v182, 2, v208\n"
      "v_and_b32 v182, 28, v182\n"
      "v_lshl_add_u32 v181, v181, 3, v182\n"
      "s_lshl_b32 s52, %[w], 7\n"
      "s_add_u32 s52, s52, 6272\n"
      "v_add_u32 v48, s52, v181\n"
      "v_lshrrev_b32 v182, 4, v205\n"
      "v_lshl_add_u32 v49, v182, 5, s52\n"
      "v_add_u32 v181, 16, v211\n"
      "s_lshl_b32 s52, %[w], 9\n"
      "v_lshl_add_u32 v128, v205, 4, s52\n"
      "s_movk_i32 s47, 12\n"
      "s_branch L_P1_%=\n"
      "L_PO_%=:\n"
      "global_load_dword v166, v208, %[qrow] sc1\n"
      "s_mov_b64 exec, s[56:57]\n"
      "global_load_dwordx2 v[40:41], v170, %[einc] sc1\n"
      "L_P1_%=:\n"
      "s_mov_b64 exec, s[54:55]\n"
      "s_waitcnt vmcnt(0)\n"
      "v_and_b32 v166, 0x1fff, v166\n"
      "v_lshrrev_b32 v182, 19, v41\n"
      "v_cmp_ne_u32 vcc, v166, v167\n"
      "v_cmp_ne_u32 s[56:57], %[nonce], v182\n"
      "s_cbranch_vccz L_NE_%=\n"
      "s_mov_b64 exec, vcc\n"
      "v_mov_b32 v167, v166\n"
      "v_lshl_or_b32 v169, v167, 19, v165\n"
      "global_store_dwordx2 v171, v[168:169], %[epub] sc1\n"
      "s_mov_b64 exec, s[54:55]\n"
      "L_NE_%=:\n"
      "s_cmp_eq_u64 s[56:57], 0\n"
      "s_cbranch_scc1 L_OK_%=\n"
      "s_sub_u32 s40, s40, 1\n"
      "s_cbranch_scc0 L_PO_%=\n"
      "v_mov_b64 v[40:41], 0\n"
      "L_OK_%=:\n"
      "global_load_dword v166, v208, %[qrow] sc1\n"
      "s_mov_b64 exec, -1\n"
      "v_and_b32 v44, 0x3f003f, v40\n"
      "v_lshrrev_b32 v45, 6, v40\n"
      "v_bfe_u32 v46, v41, 12, 6\n"
      "v_and_b32 v45, 0x3f003f, v45\n"
      "v_and_b32 v183, 0xfff, v41\n"
      "v_add3_u32 v182, v44, v45, v46\n"
      "v_add_u32_sdwa v182, v182, v182 dst_sel:DWORD dst_unused:UNUSED_PAD src0_sel:WORD_0 src1_sel:WORD_1\n"
      "v_lshl_or_b32 v182, v182, 16, v183\n"
      "s_mov_b32 exec_hi, 0\n"
      "ds_write_b32 v48, v182\n"
      "s_mov_b32 exec_hi, -1\n"
      "ds_read_b128 v[32:35], v49\n"
      "ds_read_b128 v[36:39], v49 offset:16\n"
      "v_and_b32 v183, 0x40000, v41\n"
      "v_cmp_ne_u32 vcc, 0, v183\n"
      "v_mov_b32 v40, v44\n"
      "v_mov_b32 v41, v45\n"
      "v_mov_b32 v42, v46\n"
      "v_add_u32_dpp v44, v44, v44 row_shr:1 row_mask:0xf bank_mask:0xf\n"
      "v_add_u32_dpp v45, v45, v45 row_shr:1 row_mask:0xf bank_mask:0xf\n"
      "v_add_u32_dpp v46, v46, v46 row_shr:1 row_mask:0xf bank_mask:0xf\n"
      "v_add_u32_dpp v44, v44, v44 row_shr:2 row_mask:0xf bank_mask:0xf\n"
      "v_add_u32_dpp v45, v45, v45 row_shr:2 row_mask:0xf bank_mask:0xf\n"
      "v_add_u32_dpp v46, v46, v46 row_shr:2 row_mask:0xf bank_mask:0xf\n"
      "s_waitcnt lgkmcnt(0)\n"
      "v_cmp_lt_u32_sdwa s[56:57], v211, v32 src0_sel:DWORD src1_sel:WORD_1\n"
      "v_mad_u32_u16 v48, v32, s47, v209\n"
      "s_or_b64 exec, s[56:57], 1\n"
      "buffer_load_dwordx3 v[64:66], v48, %[rec], s88 offen sc1\n"
      "s_mov_b64 exec, -1\n"
      "v_cmp_lt_u32_sdwa s[58:59], v211, v33 src0_sel:DWORD src1_sel:WORD_1\n"
      "v_mad_u32_u16 v49, v33, s47, v209\n"
      "s_or_b64 exec, s[58:59], 1\n"
      "buffer_load_dwordx3 v[68:70], v49, %[rec], s89 offen sc1\n"
      "s_mov_b64 exec, -1\n"
      "v_cmp_lt_u32_sdwa s[60:61], v211, v34 src0_sel:DWORD src1_sel:WORD_1\n"
      "v_mad_u32_u16 v50, v34, s47, v209\n"
      "s_or_b64 exec, s[60:61], 1\n"
      "buffer_load_dwordx3 v[72:74], v50, %[rec], s90 offen sc1\n"
      "s_mov_b64 exec, -1\n"
      "v_cmp_lt_u32_sdwa s[62:63], v211, v35 src0_sel:DWORD src1_sel:WORD_1\n"
      "v_mad_u32_u16 v51, v35, s47, v209\n"
      "s_or_b64 exec, s[62:63], 1\n"
      "buffer_load_dwordx3 v[76:78], v51, %[rec], s91 offen sc1\n"
      "s_mov_b64 exec, -1\n"
      "v_cmp_lt_u32_sdwa s[64:65], v211, v36 src0_sel:DWORD src1_sel:WORD_1\n"
      "v_mad_u32_u16 v52, v36, s47, v209\n"
      "s_or_b64 exec, s[64:65], 1\n"
      "buffer_load_dwordx3 v[80:82], v52, %[rec], s92 offen sc1\n"
      "s_mov_b64 exec, -1\n"
      "v_cmp_lt_u32_sdwa s[66:67], v211, v37 src0_sel:DWORD src1_sel:WORD_1\n"
      "v_mad_u32_u16 v53, v37, s47, v209\n"
      "s_or_b64 exec, s[66:67], 1\n"
      "buffer_load_dwordx3 v[84:86], v53, %[rec], s93 offen sc1\n"
      "s_mov_b64 exec, -1\n"
      "v_cmp_lt_u32_sdwa s[68:69], v211, v38 src0_sel:DWORD src1_sel:WORD_1\n"
      "v_mad_u32_u16 v54, v38, s47, v209\n"
      "s_or_b64 exec, s[68:69], 1\n"
      "buffer_load_dwordx3 v[88:90], v54, %[rec], s94 offen sc1\n"
      "s_mov_b64 exec, -1\n"
      "v_cmp_lt_u32_sdwa s[70:71], v211, v39 src0_sel:DWORD src1_sel:WORD_1\n"
      "v_mad_u32_u16 v55, v39, s47, v209\n"
      "s_or_b64 exec, s[70:71], 1\n"
      "buffer_load_dwordx3 v[92:94], v55, %[rec], s95 offen sc1\n"
      "s_mov_b64 exec, -1\n"
      "v_add_u32_dpp v44, v44, v44 row_shr:4 row_mask:0xf bank_mask:0xf\n"
      "v_add_u32_dpp v45, v45, v45 row_shr:4 row_mask:0xf bank_mask:0xf\n"
      "v_add_u32_dpp v46, v46, v46 row_shr:4 row_mask:0xf bank_mask:0xf\n"
      "v_add_u32_dpp v44, v44, v44 row_shr:8 row_mask:0xf bank_mask:0xf\n"
      "v_add_u32_dpp v45, v45, v45 row_shr:8 row_mask:0xf bank_mask:0xf\n"
      "v_add_u32_dpp v46, v46, v46 row_shr:8 row_mask:0xf bank_mask:0xf\n"
      "v_add_u32_dpp v44, v44, v44 row_bcast:15 row_mask:0xa bank_mask:0xf\n"
      "v_add_u32_dpp v45, v45, v45 row_bcast:15 row_mask:0xa bank_mask:0xf\n"
      "v_add_u32_dpp v46, v46, v46 row_bcast:15 row_mask:0xa bank_mask:0xf\n"
      "v_mov_b32 v182, 12\n"
      "s_cbranch_vccz L_NB_%=\n"
      "ds_write_b32 v182, v182 offset:0\n"
      "L_NB_%=:\n"
      "s_mov_b32 exec_lo, 0x80000000\n"
      "s_mov_b32 exec_hi, 0\n"
      "ds_add_rtn_u64 v[56:57], v199, v[44:45] offset:0\n"
      "ds_add_rtn_u32 v182, v199, v46 offset:8\n"
      "s_mov_b64 exec, -1\n"
      "v_sub_u32 v58, v44, v40\n"
      "v_sub_u32 v59, v45, v41\n"
      "v_sub_u32 v60, v46, v42\n"
      "v_cmp_lt_u32_sdwa s[72:73], v181, v32 src0_sel:DWORD src1_sel:WORD_1\n"
      "v_cmp_lt_u32_sdwa s[74:75], v181, v33 src0_sel:DWORD src1_sel:WORD_1\n"
      "v_cmp_lt_u32_sdwa s[76:77], v181, v34 src0_sel:DWORD src1_sel:WORD_1\n"
      "v_cmp_lt_u32_sdwa s[78:79], v181, v35 src0_sel:DWORD src1_sel:WORD_1\n"
      "v_cmp_lt_u32_sdwa s[80:81], v181, v36 src0_sel:DWORD src1_sel:WORD_1\n"
      "v_cmp_lt_u32_sdwa s[82:83], v181, v37 src0_sel:DWORD src1_sel:WORD_1\n"
      "v_cmp_lt_u32_sdwa s[84:85], v181, v38 src0_sel:DWORD src1_sel:WORD_1\n"
      "v_cmp_lt_u32_sdwa s[86:87], v181, v39 src0_sel:DWORD src1_sel:WORD_1\n"
      "s_waitcnt lgkmcnt(0)\n"
      "v_readlane_b32 s42, v56, 31\n"
      "v_readlane_b32 s43, v57, 31\n"
      "v_readlane_b32 s50, v182, 31\n"
      "s_nop 0\n"
      "v_add_u32 v58, s42, v58\n"
      "v_add_u32 v59, s43, v59\n"
      "v_add_u32 v60, s50, v60\n"
      "s_waitcnt vmcnt(8)\n"
      "s_mov_b64 exec, s[54:55]\n"
      "v_and_b32 v166, 0x1fff, v166\n"
      "v_cmp_ne_u32 vcc, v166, v167\n"
      "s_cbranch_vccz L_N2_A_%=\n"
      "s_mov_b64 exec, vcc\n"
      "v_lshl_or_b32 v169, v166, 19, v165\n"
      "global_store_dwordx2 v171, v[168:169], %[epub] sc1\n"
      "L_N2_A_%=:\n"
      "s_mov_b64 exec, -1\n"
      "s_barrier\n"
      "ds_read_b128 v[40:43], v199 offset:0\n"
      "s_cmp_eq_u64 s[72:73], 0\n"
      "s_cbranch_scc1 L_X0_%=\n"
      "s_mov_b64 exec, s[72:73]\n"
      "buffer_load_dwordx3 v[96:98], v48, %[rec], s88 offen offset:192 sc1\n"
      "L_X0_%=:\n"
      "s_cmp_eq_u64 s[74:75], 0\n"
      "s_cbranch_scc1 L_X1_%=\n"
      "s_mov_b64 exec, s[74:75]\n"
      "buffer_load_dwordx3 v[100:102], v49, %[rec], s89 offen offset:192 sc1\n"
      "L_X1_%=:\n"
      "s_cmp_eq_u64 s[76:77], 0\n"
      "s_cbranch_scc1 L_X2_%=\n"
      "s_mov_b64 exec, s[76:77]\n"
      "buffer_load_dwordx3 v[104:106], v50, %[rec], s90 offen offset:192 sc1\n"
      "L_X2_%=:\n"
      "s_cmp_eq_u64 s[78:79], 0\n"
      "s_cbranch_scc1 L_X3_%=\n"
      "s_mov_b64 exec, s[78:79]\n"
      "buffer_load_dwordx3 v[108:110], v51, %[rec], s91 offen offset:192 sc1\n"
      "L_X3_%=:\n"
      "s_cmp_eq_u64 s[80:81], 0\n"
      "s_cbranch_scc1 L_X4_%=\n"
      "s_mov_b64 exec, s[80:81]\n"
      "buffer_load_dwordx3 v[112:114], v52, %[rec], s92 offen offset:192 sc1\n"
      "L_X4_%=:\n"
      "s_cmp_eq_u64 s[82:83], 0\n"
      "s_cbranch_scc1 L_X5_%=\n"
      "s_mov_b64 exec, s[82:83]\n"
      "buffer_load_dwordx3 v[116:118], v53, %[rec], s93 offen offset:192 sc1\n"
      "L_X5_%=:\n"
      "s_cmp_eq_u64 s[84:85], 0\n"
      "s_cbranch_scc1 L_X6_%=\n"
      "s_mov_b64 exec, s[84:85]\n"
      "buffer_load_dwordx3 v[120:122], v54, %[rec], s94 offen offset:192 sc1\n"
      "L_X6_%=:\n"
      "s_cmp_eq_u64 s[86:87], 0\n"
      "s_cbranch_scc1 L_X7_%=\n"
      "s_mov_b64 exec, s[86:87]\n"
      "buffer_load_dwordx3 v[124:126], v55, %[rec], s95 offen offset:192 sc1\n"
      "L_X7_%=:\n"
      "s_mov_b64 exec, -1\n"
      "s_waitcnt lgkmcnt(0)\n"
      "v_readfirstlane_b32 s93, v40\n"
      "v_readfirstlane_b32 s94, v41\n"
      "v_readfirstlane_b32 s95, v42\n"
      "v_readfirstlane_b32 s96, v43\n"
      "s_and_b32 s88, s93, 0xffff\n"
      "s_lshr_b32 s89, s93, 16\n"
      "s_and_b32 s90, s94, 0xffff\n"
      "s_lshr_b32 s91, s94, 16\n"
      "s_and_b32 s92, s95, 0xffff\n"
      "s_cmp_lg_u32 s96, 0\n"
      "s_cbranch_scc1 L_SLOW_%=\n"
      "s_add_u32 s44, s88, 31\n"
      "s_lshr_b32 s44, s44, 5\n"
      "s_mov_b32 s93, s44\n"
      "s_add_u32 s88, s88, 7232\n"
      "s_add_u32 s44, s89, 31\n"
      "s_lshr_b32 s44, s44, 5\n"
      "s_add_u32 s94, s93, s44\n"
      "s_lshl_b32 s44, s93, 5\n"
      "s_add_u32 s44, s44, 7232\n"
      "s_add_u32 s89, s89, s44\n"
      "s_add_u32 s44, s90, 31\n"
      "s_lshr_b32 s44, s44, 5\n"
      "s_add_u32 s95, s94, s44\n"
      "s_lshl_b32 s44, s94, 5\n"
      "s_add_u32 s44, s44, 7232\n"
      "s_add_u32 s90, s90, s44\n"
      "s_add_u32 s44, s91, 31\n"
      "s_lshr_b32 s44, s44, 5\n"
      "s_add_u32 s96, s95, s44\n"
      "s_lshl_b32 s44, s95, 5\n"
      "s_add_u32 s44, s44, 7232\n"
      "s_add_u32 s91, s91, s44\n"
      "s_add_u32 s44, s92, 31\n"
      "s_lshr_b32 s44, s44, 5\n"
      "s_add_u32 s97, s96, s44\n"
      "s_lshl_b32 s44, s96, 5\n"
      "s_add_u32 s44, s44, 7232\n"
      "s_add_u32 s92, s92, s44\n"
      "s_cmp_gt_u32 s97, 92\n"
      "s_cbranch_scc1 L_SLOW_%=\n"
      "s_lshl_b32 s44, s93, 21\n"
      "s_add_u32 s44, s44, 473963584\n"
      "v_add_u32 v58, s44, v58\n"
      "s_lshl_b32 s44, s95, 16\n"
      "s_add_u32 s44, s44, s94\n"
      "s_lshl_b32 s44, s44, 5\n"
      "s_add_u32 s44, s44, 473963584\n"
      "v_add_u32 v59, s44, v59\n"
      "s_lshl_b32 s44, s96, 5\n"
      "s_add_u32 s44, s44, 7232\n"
      "v_add_u32 v60, s44, v60\n"
      "s_mov_b32 exec_hi, 0\n"
      "ds_write_b96 v128, v[58:60] offset:2176\n"
      "s_mov_b32 exec_hi, -1\n"
      "s_cmp_gt_u32 %[w], 5\n"
      "s_cbranch_scc1 L_NP_%=\n"
      "s_mov_b32 m0, %[w]\n"
      "s_min_u32 s40, %[w], 4\n"
      "s_movrels_b32 s44, s88\n"
      "s_sub_u32 s45, 0, s44\n"
      "s_and_b32 s45, s45, 31\n"
      "s_bfm_b64 exec, s45, 0\n"
      "s_cmp_lt_u32 %[w], 5\n"
      "s_cbranch_scc1 L_P5_%=\n"
      "s_lshl_b32 s44, s97, 5\n"
      "s_add_u32 s44, s44, 7232\n"
      "s_mov_b64 exec, -1\n"
      "L_P5_%=:\n"
      "v_add_lshl_u32 v181, s44, v205, 4\n"
      "s_mul_i32 s45, s40, 0x10000008\n"
      "s_add_u32 s45, s45, %[nb]\n"
      "v_mov_b64 v[40:41], 0\n"
      "v_mov_b32 v42, s45\n"
      "ds_write_b96 v181, v[40:42]\n"
      "s_mov_b64 exec, -1\n"
      "L_NP_%=:\n"
      "s_lshr_b32 s42, s97, 3\n"
      "s_and_b32 s43, s97, 7\n"
      "s_cmp_lt_u32 %[w], s43\n"
      "s_addc_u32 s51, s42, 0\n"
      "s_min_u32 s45, s43, %[w]\n"
      "s_mul_i32 s41, s42, %[w]\n"
      "s_add_u32 s41, s41, s45\n"
      "s_mov_b64 s[88:89], 0\n"
      "s_mov_b32 s90, 0x11111111\n"
      "s_mov_b32 s91, 0x11111111\n"
      "s_sub_i32 s44, s93, s41\n"
      "s_max_i32 s44, s44, 0\n"
      "s_lshl_b32 s45, s44, 2\n"
      "s_lshl_b64 vcc, s[90:91], s45\n"
      "s_cmp_lt_u32 s44, 16\n"
      "s_cselect_b64 vcc, vcc, 0\n"
      "s_add_u32 s88, s88, vcc_lo\n"
      "s_addc_u32 s89, s89, vcc_hi\n"
      "s_sub_i32 s44, s94, s41\n"
      "s_max_i32 s44, s44, 0\n"
      "s_lshl_b32 s45, s44, 2\n"
      "s_lshl_b64 vcc, s[90:91], s45\n"
      "s_cmp_lt_u32 s44, 16\n"
      "s_cselect_b64 vcc, vcc, 0\n"
      "s_add_u32 s88, s88, vcc_lo\n"
      "s_addc_u32 s89, s89, vcc_hi\n"
      "s_sub_i32 s44, s95, s41\n"
      "s_max_i32 s44, s44, 0\n"
      "s_lshl_b32 s45, s44, 2\n"
      "s_lshl_b64 vcc, s[90:91], s45\n"
      "s_cmp_lt_u32 s44, 16\n"
      "s_cselect_b64 vcc, vcc, 0\n"
      "s_add_u32 s88, s88, vcc_lo\n"
      "s_addc_u32 s89, s89, vcc_hi\n"
      "s_sub_i32 s44, s96, s41\n"
      "s_max_i32 s44, s44, 0\n"
      "s_lshl_b32 s45, s44, 2\n"
      "s_lshl_b64 vcc, s[90:91], s45\n"
      "s_cmp_lt_u32 s44, 16\n"
      "s_cselect_b64 vcc, vcc, 0\n"
      "s_add_u32 s88, s88, vcc_lo\n"
      "s_addc_u32 s89, s89, vcc_hi\n"
      "v_lshrrev_b32 v181, 3, v205\n"
      "v_and_b32 v182, 7, v205\n"
      "v_mov_b32 v40, 0\n"
      "v_mov_b32 v41, s93\n"
      "v_cmpx_le_u32 vcc, 1, v181\n"
      "v_mov_b32 v40, s93\n"
      "v_mov_b32 v41, s94\n"
      "v_cmpx_le_u32 vcc, 2, v181\n"
      "v_mov_b32 v40, s94\n"
      "v_mov_b32 v41, s95\n"
      "v_cmpx_le_u32 vcc, 3, v181\n"
      "v_mov_b32 v40, s95\n"
      "v_mov_b32 v41, s96\n"
      "v_cmpx_le_u32 vcc, 4, v181\n"
      "v_mov_b32 v40, s96\n"
      "v_mov_b32 v41, s97\n"
      "s_mov_b64 exec, -1\n"
      "v_min_u32 v181, s43, v182\n"
      "v_mad_u32_u24 v181, v182, s42, v181\n"
      "v_add_u32 v182, 1, v182\n"
      "v_min_u32 v183, s43, v182\n"
      "v_mad_u32_u24 v182, v182, s42, v183\n"
      "v_max_u32 v181, v181, v40\n"
      "v_min_u32 v182, v182, v41\n"
      "v_cmp_lt_u32 s[42:43], v181, v182\n"
      "s_movk_i32 s44, 2432\n"
      "v_mov_b32 v183, 64\n"
      "v_lshrrev_b64 v[40:41], v28, s[42:43]\n"
      "v_and_b32 v253, 0xff, v40\n"
      "v_ffbl_b32 v181, v253\n"
      "v_cmp_ne_u32 vcc, 0, v253\n"
      "v_add_u32 v182, -1, v253\n"
      "v_mad_u32_u24 v181, v181, s44, v29\n"
      "v_and_b32 v253, v182, v253\n"
      "v_cndmask_b32 v250, v183, v181, vcc\n"
      "v_ffbl_b32 v181, v253\n"
      "v_cmp_ne_u32 vcc, 0, v253\n"
      "v_add_u32 v182, -1, v253\n"
      "v_mad_u32_u24 v181, v181, s44, v29\n"
      "v_and_b32 v253, v182, v253\n"
      "v_cndmask_b32 v251, v183, v181, vcc\n"
      "v_ffbl_b32 v181, v253\n"
      "v_cmp_ne_u32 vcc, 0, v253\n"
      "v_add_u32 v182, -1, v253\n"
      "v_mad_u32_u24 v181, v181, s44, v29\n"
      "v_and_b32 v253, v182, v253\n"
      "v_cndmask_b32 v252, v183, v181, vcc\n"
      "v_mov_b64 v[32:33], 0\n"
      "v_mov_b64 v[34:35], 0\n"
      "v_mov_b64 v[36:37], 0\n"
      "v_mov_b64 v[38:39], 0\n"
      "v_mov_b64 v[40:41], 0\n"
      "v_mov_b64 v[42:43], 0\n"
      "v_mov_b64 v[44:45], 0\n"
      "v_mov_b64 v[46:47], 0\n"
      "v_mov_b64 v[48:49], 0\n"
      "v_mov_b64 v[50:51], 0\n"
      "v_mov_b64 v[52:53], 0\n"
      "v_mov_b64 v[54:55], 0\n"
      "v_mov_b64 v[56:57], 0\n"
      "v_mov_b64 v[58:59], 0\n"
      "v_mov_b64 v[60:61], 0\n"
      "v_mov_b64 v[62:63], 0\n"
      "v_lshlrev_b32 v180, 4, v205\n"
      "v_add_u32 v180, %[img], v180\n"
      "ds_write_b128 v180, v[40:43]\n"
      "ds_write_b128 v180, v[40:43] offset:1024\n"
      "ds_write_b128 v180, v[40:43] offset:2048\n"
      "ds_write_b128 v180, v[40:43] offset:3072\n"
      "s_mov_b64 exec, 0xffff\n"
      "ds_write_b128 v180, v[40:43] offset:4096\n"
      "s_mov_b32 s47, 0x400000\n"
      "s_mov_b32 m0, %[m0v]\n"
      "s_waitcnt vmcnt(7)\n"
      "s_mov_b64 exec, s[56:57]\n"
      "v_lshrrev_b32 v128, 28, v66\n"
      "v_lshl_add_u32 v128, v128, 1, v210\n"
      "ds_read_u16 v128, v128\n"
      "s_waitcnt vmcnt(6)\n"
      "s_mov_b64 exec, s[58:59]\n"
      "v_lshrrev_b32 v129, 28, v70\n"
      "v_lshl_add_u32 v129, v129, 1, v210\n"
      "ds_read_u16 v129, v129 offset:64\n"
      "s_mov_b64 exec, s[56:57]\n"
      "s_waitcnt lgkmcnt(1)\n"
      "v_add_u32_sdwa v128, v128, v66 dst_sel:DWORD dst_unused:UNUSED_PAD src0_sel:DWORD src1_sel:BYTE_2\n"
      "v_bfi_b32 v66, v130, s47, v66\n"
      "v_lshlrev_b32 v128, 4, v128\n"
      "ds_write_b96 v128, v[64:66]\n"
      "s_waitcnt vmcnt(5)\n"
      "s_mov_b64 exec, s[60:61]\n"
      "v_lshrrev_b32 v128, 28, v74\n"
      "v_lshl_add_u32 v128, v128, 1, v210\n"
      "ds_read_u16 v128, v128 offset:128\n"
      "s_mov_b64 exec, s[58:59]\n"
      "s_waitcnt lgkmcnt(1)\n"
      "v_add_u32_sdwa v129, v129, v70 dst_sel:DWORD dst_unused:UNUSED_PAD src0_sel:DWORD src1_sel:BYTE_2\n"
      "v_bfi_b32 v70, v130, s47, v70\n"
      "v_lshlrev_b32 v129, 4, v129\n"
      "ds_write_b96 v129, v[68:70]\n"
      "s_waitcnt vmcnt(4)\n"
      "s_mov_b64 exec, s[62:63]\n"
      "v_lshrrev_b32 v129, 28, v78\n"
      "v_lshl_add_u32 v129, v129, 1, v210\n"
      "ds_read_u16 v129, v129 offset:192\n"
      "s_mov_b64 exec, s[60:61]\n"
      "s_waitcnt lgkmcnt(1)\n"
      "v_add_u32_sdwa v128, v128, v74 dst_sel:DWORD dst_unused:UNUSED_PAD src0_sel:DWORD src1_sel:BYTE_2\n"
      "v_bfi_b32 v74, v130, s47, v74\n"
      "v_lshlrev_b32 v128, 4, v128\n"
      "ds_write_b96 v128, v[72:74]\n"
      "s_mov_b64 exec, -1\n"
      "s_mov_b64 s[56:57], s[88:89]\n"
      "s_and_b32 s58, s56, 0xff\n"
      "s_and_b32 s45, s56, 15\n"
      "s_mov_b32 s46, s45\n"
      "s_lshr_b32 s44, s58, 4\n"
      "s_lshr_b64 s[56:57], s[56:57], 8\n"
      "s_lshl_b32 s42, s41, 9\n"
      "s_add_u32 s42, s42, 115712\n"
      "s_sub_u32 s42, s42, %[img]\n"
      "s_movk_i32 s59, 3168\n"
      "s_movk_i32 s60, 2112\n"
      "s_movk_i32 s61, 1056\n"
      "v_add_u32 v181, s42, v197\n"
      "s_lshl_b32 s52, s45, 10\n"
      "v_bfi_b32 v183, v194, 0, s52\n"
      "v_add_u32 v183, v183, v201\n"
      "ds_read_b128 v[148:151], v183\n"
      "ds_read_b128 v[152:155], v183 offset:512\n"
      "s_and_b32 s52, s58, 0xf0\n"
      "v_mov_b32 v182, s52\n"
      "s_lshl_b32 s52, s58, 4\n"
      "s_and_b32 s52, s52, 0xf0\n"
      "v_bfi_b32 v182, v194, s52, v182\n"
      "v_sub_u32 v198, v197, v182\n"
      "v_mov_b64 v[164:165], 0\n"
      "v_mov_b64 v[166:167], 0\n"
      "v_mov_b64 v[168:169], 0\n"
      "v_mov_b64 v[170:171], 0\n"
      "s_setprio 1\n"
      "s_mov_b32 s40, 1\n"
      "s_cmp_gt_u32 %[w], 3\n"
      "s_cbranch_scc1 L_PR_%=\n"
      "s_setprio 2\n"
      "s_mov_b32 s40, -3\n"
      "L_PR_%=:\n"
      "s_sub_u32 s51, 0, s51\n"
      "s_waitcnt vmcnt(3)\n"
      "s_mov_b64 exec, s[64:65]\n"
      "v_lshrrev_b32 v128, 28, v82\n"
      "v_lshl_add_u32 v128, v128, 1, v210\n"
      "ds_read_u16 v128, v128 offset:256\n"
      "s_mov_b64 exec, s[62:63]\n"
      "s_waitcnt lgkmcnt(1)\n"
      "v_add_u32_sdwa v129, v129, v78 dst_sel:DWORD dst_unused:UNUSED_PAD src0_sel:DWORD src1_sel:BYTE_2\n"
      "v_bfi_b32 v78, v130, s47, v78\n"
      "v_lshlrev_b32 v129, 4, v129\n"
      "ds_write_b96 v129, v[76:78]\n"
      "s_waitcnt vmcnt(2)\n"
      "s_mov_b64 exec, s[66:67]\n"
      "v_lshrrev_b32 v129, 28, v86\n"
      "v_lshl_add_u32 v129, v129, 1, v210\n"
      "ds_read_u16 v129, v129 offset:320\n"
      "s_mov_b64 exec, s[64:65]\n"
      "s_waitcnt lgkmcnt(1)\n"
      "v_add_u32_sdwa v128, v128, v82 dst_sel:DWORD dst_unused:UNUSED_PAD src0_sel:DWORD src1_sel:BYTE_2\n"
      "v_bfi_b32 v82, v130, s47, v82\n"
      "v_lshlrev_b32 v128, 4, v128\n"
      "ds_write_b96 v128, v[80:82]\n"
      "s_waitcnt vmcnt(1)\n"
      "s_mov_b64 exec, s[68:69]\n"
      "v_lshrrev_b32 v128, 28, v90\n"
      "v_lshl_add_u32 v128, v128, 1, v210\n"
      "ds_read_u16 v128, v128 offset:384\n"
      "s_mov_b64 exec, s[66:67]\n"
      "s_waitcnt lgkmcnt(1)\n"
      "v_add_u32_sdwa v129, v129, v86 dst_sel:DWORD dst_unused:UNUSED_PAD src0_sel:DWORD src1_sel:BYTE_2\n"
      "v_bfi_b32 v86, v130, s47, v86\n"
      "v_lshlrev_b32 v129, 4, v129\n"
      "ds_write_b96 v129, v[84:86]\n"
      "s_waitcnt vmcnt(0)\n"
      "s_mov_b64 exec, s[70:71]\n"
      "v_lshrrev_b32 v129, 28, v94\n"
      "v_lshl_add_u32 v129, v129, 1, v210\n"
      "ds_read_u16 v129, v129 offset:448\n"
      "s_mov_b64 exec, s[68:69]\n"
      "s_waitcnt lgkmcnt(1)\n"
      "v_add_u32_sdwa v128, v128, v90 dst_sel:DWORD dst_unused:UNUSED_PAD src0_sel:DWORD src1_sel:BYTE_2\n"
      "v_bfi_b32 v90, v130, s47, v90\n"
      "v_lshlrev_b32 v128, 4, v128\n"
      "ds_write_b96 v128, v[88:90]\n"
      "s_mov_b64 exec, s[70:71]\n"
      "s_waitcnt lgkmcnt(0)\n"
      "v_add_u32_sdwa v129, v129, v94 dst_sel:DWORD dst_unused:UNUSED_PAD src0_sel:DWORD src1_sel:BYTE_2\n"
      "v_bfi_b32 v94, v130, s47, v94\n"
      "v_lshlrev_b32 v129, 4, v129\n"
      "ds_write_b96 v129, v[92:94]\n"
      "s_cmp_eq_u64 s[72:73], 0\n"
      "s_cbranch_scc1 L_Y0_%=\n"
      "s_mov_b64 exec, s[72:73]\n"
      "v_lshrrev_b32 v128, 28, v98\n"
      "v_lshl_add_u32 v128, v128, 1, v210\n"
      "ds_read_u16 v128, v128\n"
      "s_waitcnt lgkmcnt(0)\n"
      "v_add_u32_sdwa v128, v128, v98 dst_sel:DWORD dst_unused:UNUSED_PAD src0_sel:DWORD src1_sel:BYTE_2\n"
      "v_bfi_b32 v98, v130, s47, v98\n"
      "v_lshlrev_b32 v128, 4, v128\n"
      "ds_write_b96 v128, v[96:98]\n"
      "L_Y0_%=:\n"
      "s_cmp_eq_u64 s[74:75], 0\n"
      "s_cbranch_scc1 L_Y1_%=\n"
      "s_mov_b64 exec, s[74:75]\n"
      "v_lshrrev_b32 v128, 28, v102\n"
      "v_lshl_add_u32 v128, v128, 1, v210\n"
      "ds_read_u16 v128, v128 offset:64\n"
      "s_waitcnt lgkmcnt(0)\n"
      "v_add_u32_sdwa v128, v128, v102 dst_sel:DWORD dst_unused:UNUSED_PAD src0_sel:DWORD src1_sel:BYTE_2\n"
      "v_bfi_b32 v102, v130, s47, v102\n"
      "v_lshlrev_b32 v128, 4, v128\n"
      "ds_write_b96 v128, v[100:102]\n"
      "L_Y1_%=:\n"
      "s_cmp_eq_u64 s[76:77], 0\n"
      "s_cbranch_scc1 L_Y2_%=\n"
      "s_mov_b64 exec, s[76:77]\n"
      "v_lshrrev_b32 v128, 28, v106\n"
      "v_lshl_add_u32 v128, v128, 1, v210\n"
      "ds_read_u16 v128, v128 offset:128\n"
      "s_waitcnt lgkmcnt(0)\n"
      "v_add_u32_sdwa v128, v128, v106 dst_sel:DWORD dst_unused:UNUSED_PAD src0_sel:DWORD src1_sel:BYTE_2\n"
      "v_bfi_b32 v106, v130, s47, v106\n"
      "v_lshlrev_b32 v128, 4, v128\n"
      "ds_write_b96 v128, v[104:106]\n"
      "L_Y2_%=:\n"
      "s_cmp_eq_u64 s[78:79], 0\n"
      "s_cbranch_scc1 L_Y3_%=\n"
      "s_mov_b64 exec, s[78:79]\n"
      "v_lshrrev_b32 v128, 28, v110\n"
      "v_lshl_add_u32 v128, v128, 1, v210\n"
      "ds_read_u16 v128, v128 offset:192\n"
      "s_waitcnt lgkmcnt(0)\n"
      "v_add_u32_sdwa v128, v128, v110 dst_sel:DWORD dst_unused:UNUSED_PAD src0_sel:DWORD src1_sel:BYTE_2\n"
      "v_bfi_b32 v110, v130, s47, v110\n"
      "v_lshlrev_b32 v128, 4, v128\n"
      "ds_write_b96 v128, v[108:110]\n"
      "L_Y3_%=:\n"
      "s_cmp_eq_u64 s[80:81], 0\n"
      "s_cbranch_scc1 L_Y4_%=\n"
      "s_mov_b64 exec, s[80:81]\n"
      "v_lshrrev_b32 v128, 28, v114\n"
      "v_lshl_add_u32 v128, v128, 1, v210\n"
      "ds_read_u16 v128, v128 offset:256\n"
      "s_waitcnt lgkmcnt(0)\n"
      "v_add_u32_sdwa v128, v128, v114 dst_sel:DWORD dst_unused:UNUSED_PAD src0_sel:DWORD src1_sel:BYTE_2\n"
      "v_bfi_b32 v114, v130, s47, v114\n"
      "v_lshlrev_b32 v128, 4, v128\n"
      "ds_write_b96 v128, v[112:114]\n"
      "L_Y4_%=:\n"
      "s_cmp_eq_u64 s[82:83], 0\n"
      "s_cbranch_scc1 L_Y5_%=\n"
      "s_mov_b64 exec, s[82:83]\n"
      "v_lshrrev_b32 v128, 28, v118\n"
      "v_lshl_add_u32 v128, v128, 1, v210\n"
      "ds_read_u16 v128, v128 offset:320\n"
      "s_waitcnt lgkmcnt(0)\n"
      "v_add_u32_sdwa v128, v128, v118 dst_sel:DWORD dst_unused:UNUSED_PAD src0_sel:DWORD src1_sel:BYTE_2\n"
      "v_bfi_b32 v118, v130, s47, v118\n"
      "v_lshlrev_b32 v128, 4, v128\n"
      "ds_write_b96 v128, v[116:118]\n"
      "L_Y5_%=:\n"
      "s_cmp_eq_u64 s[84:85], 0\n"
      "s_cbranch_scc1 L_Y6_%=\n"
      "s_mov_b64 exec, s[84:85]\n"
      "v_lshrrev_b32 v128, 28, v122\n"
      "v_lshl_add_u32 v128, v128, 1, v210\n"
      "ds_read_u16 v128, v128 offset:384\n"
      "s_waitcnt lgkmcnt(0)\n"
      "v_add_u32_sdwa v128, v128, v122 dst_sel:DWORD dst_unused:UNUSED_PAD src0_sel:DWORD src1_sel:BYTE_2\n"
      "v_bfi_b32 v122, v130, s47, v122\n"
      "v_lshlrev_b32 v128, 4, v128\n"
      "ds_write_b96 v128, v[120:122]\n"
      "L_Y6_%=:\n"
      "s_cmp_eq_u64 s[86:87], 0\n"
      "s_cbranch_scc1 L_Y7_%=\n"
      "s_mov_b64 exec, s[86:87]\n"
      "v_lshrrev_b32 v128, 28, v126\n"
      "v_lshl_add_u32 v128, v128, 1, v210\n"
      "ds_read_u16 v128, v128 offset:448\n"
      "s_waitcnt lgkmcnt(0)\n"
      "v_add_u32_sdwa v128, v128, v126 dst_sel:DWORD dst_unused:UNUSED_PAD src0_sel:DWORD src1_sel:BYTE_2\n"
      "v_bfi_b32 v126, v130, s47, v126\n"
      "v_lshlrev_b32 v128, 4, v128\n"
      "ds_write_b96 v128, v[124:126]\n"
      "L_Y7_%=:\n"
      "s_mov_b64 exec, -1\n"
      "v_mov_b64 v[120:121], 0\n"
      "v_mov_b64 v[122:123], 0\n"
      "v_mov_b64 v[124:125], 0\n"
      "v_mov_b64 v[126:127], 0\n"
      "s_waitcnt lgkmcnt(0)\n"
      "s_barrier\n"
      "s_cmp_eq_u32 s51, 0\n"
      "s_cbranch_scc1 L_END_%=\n"
      "ds_read_b96 v[176:178], v181\n"
      "s_add_u32 s42, s42, 1024\n"
      "s_waitcnt lgkmcnt(0)\n"
      "v_mad_u32_u16 v181, v178, s49, v207\n"
      "v_lshlrev_b32_sdwa v182, 4, v178 dst_sel:DWORD dst_unused:UNUSED_PAD src0_sel:DWORD src1_sel:WORD_0\n"
      "ds_write_b32 v206, v181\n"
      "ds_read_b128 v[172:175], v196\n"
      "global_load_dwordx3 v[184:186], v182, %[p4] sc1\n"
      "v_lshlrev_b32_sdwa v183, 3, v178 dst_sel:DWORD dst_unused:UNUSED_PAD src0_sel:DWORD src1_sel:BYTE_3\n"
      "ds_read_b96 v[188:190], v183 offset:256\n"
      "s_waitcnt lgkmcnt(1)\n"
      "buffer_load_dwordx4 v172, %[bp], s59 offen sc1 lds\n"
      "buffer_load_dwordx4 v173, %[bp], s60 offen offset:1056 sc1 lds\n"
      "buffer_load_dwordx4 v174, %[bp], s61 offen offset:2112 sc1 lds\n"
      "buffer_load_dwordx4 v175, %[bp], 0 offen offset:3168 sc1 lds\n"
      "v_add_u32_sdwa v180, v198, v178 dst_sel:DWORD dst_unused:UNUSED_PAD src0_sel:DWORD src1_sel:BYTE_3\n"
      "ds_read_b128 v[172:175], v196 offset:128\n"
      "ds_write_b8_d16_hi v180, v178 offset:1\n"
      "s_waitcnt vmcnt(4) lgkmcnt(2)\n"
      "v_pk_add_f32 v[188:189], v[188:189], v[184:185] neg_lo:[0,1] neg_hi:[0,1]\n"
      "v_sub_f32 v190, v190, v186\n"
      "v_cvt_pk_bf16_f32 v187, 0, v188\n"
      "v_cvt_pk_bf16_f32 v179, v189, v190\n"
      "v_dot2_f32_bf16 v187, v187, v187, 0\n"
      "v_dot2_f32_bf16 v187, v179, v179, v187\n"
      "s_nop 2\n"
      "v_mul_f32 v187, s53, v187\n"
      "v_cvt_pk_bf16_f32 v178, v187, v188\n"
      "ds_write_b16 v180, v179 offset:2176\n"
      "ds_write_b16_d16_hi v180, v179 offset:3264\n"
      "ds_write_b16_d16_hi v180, v178 offset:1088\n"
      "ds_write_b128 v200, v[176:179]\n"
      "ds_read_b128 v[144:147], v193\n"
      "v_add_u32 v181, s42, v197\n"
      "ds_read_b96 v[176:178], v181\n"
      "s_add_u32 s42, s42, 1024\n"
      "s_waitcnt vmcnt(0)\n"
      "ds_read_b128 v[128:131], v195\n"
      "ds_read_b128 v[132:135], v195 offset:32\n"
      "ds_read_b128 v[136:139], v195 offset:64\n"
      "ds_read_b128 v[140:143], v195 offset:96\n"
      "s_waitcnt lgkmcnt(0)\n"
      "v_mfma_f32_32x32x16_bf16 v[64:79], v[144:147], v[148:151], 0\n"
      "buffer_load_dwordx4 v172, %[bp], s59 offen sc1 lds\n"
      "buffer_load_dwordx4 v173, %[bp], s60 offen offset:1056 sc1 lds\n"
      "v_mfma_f32_32x32x16_bf16 v[64:79], v[128:131], v[156:159], v[64:79]\n"
      "buffer_load_dwordx4 v174, %[bp], s61 offen offset:2112 sc1 lds\n"
      "buffer_load_dwordx4 v175, %[bp], 0 offen offset:3168 sc1 lds\n"
      "v_mfma_f32_32x32x16_bf16 v[64:79], v[132:135], v[160:163], v[64:79]\n"
      "s_nop 7\n"
      "s_nop 7\n"
      "L_B_%=:\n"
      "v_mfma_f32_32x32x16_bf16 v[48:63], v[120:123], v[164:167], v[48:63]\n"
      "v_exp_f32 v96, v64\n"
      "s_waitcnt lgkmcnt(0)\n"
      "v_lshlrev_b32_sdwa v182, 4, v178 dst_sel:DWORD dst_unused:UNUSED_PAD src0_sel:DWORD src1_sel:WORD_0\n"
      "v_mad_u32_u16 v181, v178, s49, v207\n"
      "v_exp_f32 v97, v65\n"
      "global_load_dwordx3 v[184:186], v182, %[p4] sc1\n"
      "ds_write_b32 v206, v181\n"
      "v_exp_f32 v98, v66\n"
      "v_lshlrev_b32_sdwa v183, 3, v178 dst_sel:DWORD dst_unused:UNUSED_PAD src0_sel:DWORD src1_sel:BYTE_3\n"
      "ds_read_b96 v[188:190], v183 offset:256\n"
      "v_exp_f32 v99, v67\n"
      "v_mfma_f32_32x32x16_bf16 v[48:63], v[124:127], v[168:171], v[48:63]\n"
      "v_exp_f32 v100, v68\n"
      "ds_read_b64_tr_b16 v[164:165], v192\n"
      "ds_read_b64_tr_b16 v[166:167], v192 offset:128\n"
      "v_exp_f32 v101, v69\n"
      "ds_read_b64_tr_b16 v[168:169], v192 offset:256\n"
      "ds_read_b64_tr_b16 v[170:171], v192 offset:384\n"
      "v_exp_f32 v102, v70\n"
      "v_exp_f32 v103, v71\n"
      "v_mfma_f32_32x32x16_bf16 v[80:95], v[144:147], v[152:155], 0\n"
      "s_cmp_eq_u32 s44, s45\n"
      "s_cbranch_scc0 L_CWE_%=\n"
      "L_RWE_%=:\n"
      "v_exp_f32 v104, v72\n"
      "v_exp_f32 v105, v73\n"
      "ds_read_b128 v[144:147], v193 offset:512\n"
      "v_exp_f32 v106, v74\n"
      "v_exp_f32 v107, v75\n"
      "ds_read_b128 v[172:175], v196\n"
      "v_exp_f32 v108, v76\n"
      "v_exp_f32 v109, v77\n"
      "v_exp_f32 v110, v78\n"
      "v_exp_f32 v111, v79\n"
      "v_pk_add_f32 v[96:97], v[96:97], 1.0 op_sel_hi:[1,0]\n"
      "v_pk_add_f32 v[98:99], v[98:99], 1.0 op_sel_hi:[1,0]\n"
      "v_pk_add_f32 v[100:101], v[100:101], 1.0 op_sel_hi:[1,0]\n"
      "v_pk_add_f32 v[102:103], v[102:103], 1.0 op_sel_hi:[1,0]\n"
      "v_pk_add_f32 v[104:105], v[104:105], 1.0 op_sel_hi:[1,0]\n"
      "v_pk_add_f32 v[106:107], v[106:107], 1.0 op_sel_hi:[1,0]\n"
      "v_pk_add_f32 v[108:109], v[108:109], 1.0 op_sel_hi:[1,0]\n"
      "v_pk_add_f32 v[110:111], v[110:111], 1.0 op_sel_hi:[1,0]\n"
      "v_mfma_f32_32x32x16_bf16 v[80:95], v[136:139], v[156:159], v[80:95]\n"
      "v_rcp_f32 v96, v96\n"
      "v_rcp_f32 v97, v97\n"
      "v_rcp_f32 v98, v98\n"
      "v_rcp_f32 v99, v99\n"
      "v_mfma_f32_32x32x16_bf16 v[80:95], v[140:143], v[160:163], v[80:95]\n"
      "v_rcp_f32 v100, v100\n"
      "v_rcp_f32 v101, v101\n"
      "v_rcp_f32 v102, v102\n"
      "v_rcp_f32 v103, v103\n"
      "v_rcp_f32 v104, v104\n"
      "v_rcp_f32 v105, v105\n"
      "s_waitcnt vmcnt(1)\n"
      "ds_read_b128 v[128:131], v195\n"
      "v_rcp_f32 v106, v106\n"
      "ds_read_b128 v[132:135], v195 offset:32\n"
      "v_rcp_f32 v107, v107\n"
      "ds_read_b128 v[136:139], v195 offset:64\n"
      "v_rcp_f32 v108, v108\n"
      "ds_read_b128 v[140:143], v195 offset:96\n"
      "v_rcp_f32 v109, v109\n"
      "v_rcp_f32 v110, v110\n"
      "v_rcp_f32 v111, v111\n"
      "v_pk_mul_f32 v[96:97], v[64:65], v[96:97]\n"
      "v_pk_mul_f32 v[98:99], v[66:67], v[98:99]\n"
      "v_pk_mul_f32 v[100:101], v[68:69], v[100:101]\n"
      "v_pk_mul_f32 v[102:103], v[70:71], v[102:103]\n"
      "v_pk_mul_f32 v[104:105], v[72:73], v[104:105]\n"
      "v_pk_mul_f32 v[106:107], v[74:75], v[106:107]\n"
      "v_pk_mul_f32 v[108:109], v[76:77], v[108:109]\n"
      "v_pk_mul_f32 v[110:111], v[78:79], v[110:111]\n"
      "v_cvt_pk_bf16_f32 v112, v96, v97\n"
      "v_cvt_pk_bf16_f32 v113, v98, v99\n"
      "v_cvt_pk_bf16_f32 v114, v100, v101\n"
      "v_cvt_pk_bf16_f32 v115, v102, v103\n"
      "v_cvt_pk_bf16_f32 v116, v104, v105\n"
      "v_cvt_pk_bf16_f32 v117, v106, v107\n"
      "v_cvt_pk_bf16_f32 v118, v108, v109\n"
      "v_cvt_pk_bf16_f32 v119, v110, v111\n"
      "s_cmp_eq_u32 s46, s45\n"
      "s_cbranch_scc0 L_CFE_%=\n"
      "L_RFE_%=:\n"
      "s_add_u32 s51, s51, 1\n"
      "s_cbranch_scc1 L_D_%=\n"
      "s_cmp_lg_u32 s51, s40\n"
      "s_cbranch_scc0 L_CPE_%=\n"
      "L_RPE_%=:\n"
      "s_waitcnt lgkmcnt(4)\n"
      "v_mfma_f32_32x32x16_bf16 v[32:47], v[112:115], v[164:167], v[32:47]\n"
      "v_exp_f32 v96, v80\n"
      "s_waitcnt lgkmcnt(0)\n"
      "buffer_load_dwordx4 v172, %[bp], s59 offen sc1 lds\n"
      "v_exp_f32 v97, v81\n"
      "buffer_load_dwordx4 v173, %[bp], s60 offen offset:1056 sc1 lds\n"
      "v_exp_f32 v98, v82\n"
      "buffer_load_dwordx4 v174, %[bp], s61 offen offset:2112 sc1 lds\n"
      "v_exp_f32 v99, v83\n"
      "v_mfma_f32_32x32x16_bf16 v[32:47], v[116:119], v[168:171], v[32:47]\n"
      "v_exp_f32 v100, v84\n"
      "buffer_load_dwordx4 v175, %[bp], 0 offen offset:3168 sc1 lds\n"
      "v_exp_f32 v101, v85\n"
      "s_mov_b32 s46, s45\n"
      "s_mov_b32 s45, s44\n"
      "v_exp_f32 v102, v86\n"
      "s_and_b32 s44, s56, 15\n"
      "v_exp_f32 v103, v87\n"
      "v_mfma_f32_32x32x16_bf16 v[64:79], v[144:147], v[148:151], 0\n"
      "v_exp_f32 v104, v88\n"
      "v_exp_f32 v105, v89\n"
      "v_exp_f32 v106, v90\n"
      "v_exp_f32 v107, v91\n"
      "v_exp_f32 v108, v92\n"
      "v_exp_f32 v109, v93\n"
      "ds_read_b128 v[172:175], v196 offset:128\n"
      "v_exp_f32 v110, v94\n"
      "s_waitcnt vmcnt(4)\n"
      "v_exp_f32 v111, v95\n"
      "v_pk_add_f32 v[188:189], v[188:189], v[184:185] neg_lo:[0,1] neg_hi:[0,1]\n"
      "v_sub_f32 v190, v190, v186\n"
      "v_cvt_pk_bf16_f32 v187, 0, v188\n"
      "v_cvt_pk_bf16_f32 v179, v189, v190\n"
      "v_dot2_f32_bf16 v187, v187, v187, 0\n"
      "v_dot2_f32_bf16 v187, v179, v179, v187\n"
      "v_pk_add_f32 v[96:97], v[96:97], 1.0 op_sel_hi:[1,0]\n"
      "v_pk_add_f32 v[98:99], v[98:99], 1.0 op_sel_hi:[1,0]\n"
      "v_pk_add_f32 v[100:101], v[100:101], 1.0 op_sel_hi:[1,0]\n"
      "v_pk_add_f32 v[102:103], v[102:103], 1.0 op_sel_hi:[1,0]\n"
      "v_pk_add_f32 v[104:105], v[104:105], 1.0 op_sel_hi:[1,0]\n"
      "v_pk_add_f32 v[106:107], v[106:107], 1.0 op_sel_hi:[1,0]\n"
      "v_pk_add_f32 v[108:109], v[108:109], 1.0 op_sel_hi:[1,0]\n"
      "v_pk_add_f32 v[110:111], v[110:111], 1.0 op_sel_hi:[1,0]\n"
      "v_mfma_f32_32x32x16_bf16 v[64:79], v[128:131], v[156:159], v[64:79]\n"
      "v_rcp_f32 v96, v96\n"
      "v_rcp_f32 v97, v97\n"
      "v_rcp_f32 v98, v98\n"
      "v_rcp_f32 v99, v99\n"
      "v_mfma_f32_32x32x16_bf16 v[64:79], v[132:135], v[160:163], v[64:79]\n"
      "v_rcp_f32 v100, v100\n"
      "v_rcp_f32 v101, v101\n"
      "v_rcp_f32 v102, v102\n"
      "v_rcp_f32 v103, v103\n"
      "v_rcp_f32 v104, v104\n"
      "v_rcp_f32 v105, v105\n"
      "v_rcp_f32 v106, v106\n"
      "v_rcp_f32 v107, v107\n"
      "v_rcp_f32 v108, v108\n"
      "v_rcp_f32 v109, v109\n"
      "v_rcp_f32 v110, v110\n"
      "v_rcp_f32 v111, v111\n"
      "v_mul_f32 v187, s53, v187\n"
      "v_pk_mul_f32 v[96:97], v[80:81], v[96:97]\n"
      "v_pk_mul_f32 v[98:99], v[82:83], v[98:99]\n"
      "v_pk_mul_f32 v[100:101], v[84:85], v[100:101]\n"
      "v_pk_mul_f32 v[102:103], v[86:87], v[102:103]\n"
      "v_pk_mul_f32 v[104:105], v[88:89], v[104:105]\n"
      "v_pk_mul_f32 v[106:107], v[90:91], v[106:107]\n"
      "v_pk_mul_f32 v[108:109], v[92:93], v[108:109]\n"
      "v_pk_mul_f32 v[110:111], v[94:95], v[110:111]\n"
      "v_cvt_pk_bf16_f32 v120, v96, v97\n"
      "v_cvt_pk_bf16_f32 v121, v98, v99\n"
      "v_cvt_pk_bf16_f32 v122, v100, v101\n"
      "v_cvt_pk_bf16_f32 v123, v102, v103\n"
      "v_cvt_pk_bf16_f32 v124, v104, v105\n"
      "v_cvt_pk_bf16_f32 v125, v106, v107\n"
      "v_cvt_pk_bf16_f32 v126, v108, v109\n"
      "v_cvt_pk_bf16_f32 v127, v110, v111\n"
      "v_mfma_f32_32x32x16_bf16 v[48:63], v[120:123], v[164:167], v[48:63]\n"
      "v_exp_f32 v96, v64\n"
      "v_exp_f32 v97, v65\n"
      "s_and_b32 s52, s56, 0xff\n"
      "v_exp_f32 v98, v66\n"
      "v_exp_f32 v99, v67\n"
      "v_mfma_f32_32x32x16_bf16 v[48:63], v[124:127], v[168:171], v[48:63]\n"
      "v_exp_f32 v100, v68\n"
      "ds_read_b64_tr_b16 v[164:165], v192 offset:512\n"
      "ds_read_b64_tr_b16 v[166:167], v192 offset:640\n"
      "v_exp_f32 v101, v69\n"
      "ds_read_b64_tr_b16 v[168:169], v192 offset:768\n"
      "ds_read_b64_tr_b16 v[170:171], v192 offset:896\n"
      "v_exp_f32 v102, v70\n"
      "ds_write_b16 v180, v199\n"
      "ds_write_b16 v180, v199 offset:1088\n"
      "v_exp_f32 v103, v71\n"
      "v_mfma_f32_32x32x16_bf16 v[80:95], v[144:147], v[152:155], 0\n"
      "s_cmp_eq_u32 s44, s45\n"
      "s_cbranch_scc0 L_CWO_%=\n"
      "L_RWO_%=:\n"
      "v_exp_f32 v104, v72\n"
      "ds_write_b16 v180, v199 offset:2176\n"
      "ds_write_b16 v180, v199 offset:3264\n"
      "v_exp_f32 v105, v73\n"
      "s_cmp_eq_u32 s52, s58\n"
      "s_cbranch_scc0 L_CI_%=\n"
      "L_RI_%=:\n"
      "v_add_u32_sdwa v180, v198, v178 dst_sel:DWORD dst_unused:UNUSED_PAD src0_sel:DWORD src1_sel:BYTE_3\n"
      "v_exp_f32 v106, v74\n"
      "ds_write_b8_d16_hi v180, v178 offset:1\n"
      "v_cvt_pk_bf16_f32 v178, v187, v188\n"
      "v_exp_f32 v107, v75\n"
      "ds_write_b16 v180, v179 offset:2176\n"
      "ds_write_b16_d16_hi v180, v179 offset:3264\n"
      "v_exp_f32 v108, v76\n"
      "ds_write_b16_d16_hi v180, v178 offset:1088\n"
      "ds_write_b128 v200, v[176:179]\n"
      "v_exp_f32 v109, v77\n"
      "ds_read_b128 v[144:147], v193\n"
      "v_exp_f32 v110, v78\n"
      "v_exp_f32 v111, v79\n"
      "v_pk_add_f32 v[96:97], v[96:97], 1.0 op_sel_hi:[1,0]\n"
      "v_pk_add_f32 v[98:99], v[98:99], 1.0 op_sel_hi:[1,0]\n"
      "v_pk_add_f32 v[100:101], v[100:101], 1.0 op_sel_hi:[1,0]\n"
      "v_pk_add_f32 v[102:103], v[102:103], 1.0 op_sel_hi:[1,0]\n"
      "v_pk_add_f32 v[104:105], v[104:105], 1.0 op_sel_hi:[1,0]\n"
      "v_pk_add_f32 v[106:107], v[106:107], 1.0 op_sel_hi:[1,0]\n"
      "v_pk_add_f32 v[108:109], v[108:109], 1.0 op_sel_hi:[1,0]\n"
      "v_pk_add_f32 v[110:111], v[110:111], 1.0 op_sel_hi:[1,0]\n"
      "v_mfma_f32_32x32x16_bf16 v[80:95], v[136:139], v[156:159], v[80:95]\n"
      "v_rcp_f32 v96, v96\n"
      "v_rcp_f32 v97, v97\n"
      "v_rcp_f32 v98, v98\n"
      "v_rcp_f32 v99, v99\n"
      "v_mfma_f32_32x32x16_bf16 v[80:95], v[140:143], v[160:163], v[80:95]\n"
      "v_rcp_f32 v100, v100\n"
      "v_rcp_f32 v101, v101\n"
      "v_rcp_f32 v102, v102\n"
      "v_rcp_f32 v103, v103\n"
      "v_rcp_f32 v104, v104\n"
      "v_rcp_f32 v105, v105\n"
      "s_waitcnt vmcnt(0)\n"
      "ds_read_b128 v[128:131], v195\n"
      "v_rcp_f32 v106, v106\n"
      "ds_read_b128 v[132:135], v195 offset:32\n"
      "v_rcp_f32 v107, v107\n"
      "ds_read_b128 v[136:139], v195 offset:64\n"
      "v_rcp_f32 v108, v108\n"
      "ds_read_b128 v[140:143], v195 offset:96\n"
      "v_rcp_f32 v109, v109\n"
      "v_rcp_f32 v110, v110\n"
      "v_rcp_f32 v111, v111\n"
      "v_pk_mul_f32 v[96:97], v[64:65], v[96:97]\n"
      "v_pk_mul_f32 v[98:99], v[66:67], v[98:99]\n"
      "v_pk_mul_f32 v[100:101], v[68:69], v[100:101]\n"
      "v_pk_mul_f32 v[102:103], v[70:71], v[102:103]\n"
      "v_pk_mul_f32 v[104:105], v[72:73], v[104:105]\n"
      "v_pk_mul_f32 v[106:107], v[74:75], v[106:107]\n"
      "v_pk_mul_f32 v[108:109], v[76:77], v[108:109]\n"
      "v_pk_mul_f32 v[110:111], v[78:79], v[110:111]\n"
      "v_cvt_pk_bf16_f32 v112, v96, v97\n"
      "v_cvt_pk_bf16_f32 v113, v98, v99\n"
      "v_cvt_pk_bf16_f32 v114, v100, v101\n"
      "v_cvt_pk_bf16_f32 v115, v102, v103\n"
      "v_cvt_pk_bf16_f32 v116, v104, v105\n"
      "v_cvt_pk_bf16_f32 v117, v106, v107\n"
      "v_cvt_pk_bf16_f32 v118, v108, v109\n"
      "v_cvt_pk_bf16_f32 v119, v110, v111\n"
      "s_cmp_eq_u32 s46, s45\n"
      "s_cbranch_scc0 L_CFO_%=\n"
      "L_RFO_%=:\n"
      "s_add_u32 s51, s51, 1\n"
      "s_cbranch_scc1 L_D_%=\n"
      "s_cmp_lg_u32 s51, s40\n"
      "s_cbranch_scc0 L_CPO_%=\n"
      "L_RPO_%=:\n"
      "s_waitcnt lgkmcnt(4)\n"
      "v_mfma_f32_32x32x16_bf16 v[32:47], v[112:115], v[164:167], v[32:47]\n"
      "v_exp_f32 v96, v80\n"
      "s_waitcnt lgkmcnt(0)\n"
      "buffer_load_dwordx4 v172, %[bp], s59 offen sc1 lds\n"
      "v_exp_f32 v97, v81\n"
      "buffer_load_dwordx4 v173, %[bp], s60 offen offset:1056 sc1 lds\n"
      "v_exp_f32 v98, v82\n"
      "buffer_load_dwordx4 v174, %[bp], s61 offen offset:2112 sc1 lds\n"
      "v_exp_f32 v99, v83\n"
      "v_mfma_f32_32x32x16_bf16 v[32:47], v[116:119], v[168:171], v[32:47]\n"
      "v_exp_f32 v100, v84\n"
      "buffer_load_dwordx4 v175, %[bp], 0 offen offset:3168 sc1 lds\n"
      "v_exp_f32 v101, v85\n"
      "s_mov_b32 s46, s45\n"
      "s_mov_b32 s45, s44\n"
      "v_exp_f32 v102, v86\n"
      "s_lshr_b32 s44, s52, 4\n"
      "s_lshr_b64 s[56:57], s[56:57], 8\n"
      "v_exp_f32 v103, v87\n"
      "v_mfma_f32_32x32x16_bf16 v[64:79], v[144:147], v[148:151], 0\n"
      "v_exp_f32 v104, v88\n"
      "v_exp_f32 v105, v89\n"
      "v_exp_f32 v106, v90\n"
      "v_exp_f32 v107, v91\n"
      "v_exp_f32 v108, v92\n"
      "v_exp_f32 v109, v93\n"
      "v_add_u32 v181, s42, v197\n"
      "ds_read_b96 v[176:178], v181\n"
      "s_add_u32 s42, s42, 1024\n"
      "v_exp_f32 v110, v94\n"
      "v_exp_f32 v111, v95\n"
      "v_pk_add_f32 v[96:97], v[96:97], 1.0 op_sel_hi:[1,0]\n"
      "v_pk_add_f32 v[98:99], v[98:99], 1.0 op_sel_hi:[1,0]\n"
      "v_pk_add_f32 v[100:101], v[100:101], 1.0 op_sel_hi:[1,0]\n"
      "v_pk_add_f32 v[102:103], v[102:103], 1.0 op_sel_hi:[1,0]\n"
      "v_pk_add_f32 v[104:105], v[104:105], 1.0 op_sel_hi:[1,0]\n"
      "v_pk_add_f32 v[106:107], v[106:107], 1.0 op_sel_hi:[1,0]\n"
      "v_pk_add_f32 v[108:109], v[108:109], 1.0 op_sel_hi:[1,0]\n"
      "v_pk_add_f32 v[110:111], v[110:111], 1.0 op_sel_hi:[1,0]\n"
      "v_mfma_f32_32x32x16_bf16 v[64:79], v[128:131], v[156:159], v[64:79]\n"
      "v_rcp_f32 v96, v96\n"
      "v_rcp_f32 v97, v97\n"
      "v_rcp_f32 v98, v98\n"
      "v_rcp_f32 v99, v99\n"
      "v_mfma_f32_32x32x16_bf16 v[64:79], v[132:135], v[160:163], v[64:79]\n"
      "v_rcp_f32 v100, v100\n"
      "v_rcp_f32 v101, v101\n"
      "v_rcp_f32 v102, v102\n"
      "v_rcp_f32 v103, v103\n"
      "v_rcp_f32 v104, v104\n"
      "v_rcp_f32 v105, v105\n"
      "v_rcp_f32 v106, v106\n"
      "v_rcp_f32 v107, v107\n"
      "v_rcp_f32 v108, v108\n"
      "v_rcp_f32 v109, v109\n"
      "v_rcp_f32 v110, v110\n"
      "v_rcp_f32 v111, v111\n"
      "v_pk_mul_f32 v[96:97], v[80:81], v[96:97]\n"
      "v_pk_mul_f32 v[98:99], v[82:83], v[98:99]\n"
      "v_pk_mul_f32 v[100:101], v[84:85], v[100:101]\n"
      "v_pk_mul_f32 v[102:103], v[86:87], v[102:103]\n"
      "v_pk_mul_f32 v[104:105], v[88:89], v[104:105]\n"
      "v_pk_mul_f32 v[106:107], v[90:91], v[106:107]\n"
      "v_pk_mul_f32 v[108:109], v[92:93], v[108:109]\n"
      "v_pk_mul_f32 v[110:111], v[94:95], v[110:111]\n"
      "v_cvt_pk_bf16_f32 v120, v96, v97\n"
      "v_cvt_pk_bf16_f32 v121, v98, v99\n"
      "v_cvt_pk_bf16_f32 v122, v100, v101\n"
      "v_cvt_pk_bf16_f32 v123, v102, v103\n"
      "v_cvt_pk_bf16_f32 v124, v104, v105\n"
      "v_cvt_pk_bf16_f32 v125, v106, v107\n"
      "v_cvt_pk_bf16_f32 v126, v108, v109\n"
      "v_cvt_pk_bf16_f32 v127, v110, v111\n"
      "s_branch L_B_%=\n"
      "L_D_%=:\n"
      "s_setprio 0\n"
      "s_waitcnt lgkmcnt(0)\n"
      "v_mfma_f32_32x32x16_bf16 v[32:47], v[112:115], v[164:167], v[32:47]\n"
      "v_exp_f32 v96, v80\n"
      "v_exp_f32 v97, v81\n"
      "v_exp_f32 v98, v82\n"
      "v_exp_f32 v99, v83\n"
      "v_mfma_f32_32x32x16_bf16 v[32:47], v[116:119], v[168:171], v[32:47]\n"
      "v_exp_f32 v100, v84\n"
      "ds_read_b128 v[128:131], v202\n"
      "ds_read_b128 v[132:135], v202 offset:32\n"
      "v_exp_f32 v101, v85\n"
      "ds_read_b128 v[136:139], v202 offset:64\n"
      "ds_read_b128 v[140:143], v202 offset:96\n"
      "v_exp_f32 v102, v86\n"
      "ds_read_b128 v[144:147], v202 offset:128\n"
      "ds_read_b128 v[148:151], v202 offset:160\n"
      "v_exp_f32 v103, v87\n"
      "ds_read_b128 v[152:155], v202 offset:192\n"
      "ds_read_b128 v[172:175], v202 offset:224\n"
      "v_exp_f32 v104, v88\n"
      "s_mul_i32 s47, s45, 2432\n"
      "s_add_u32 s47, s47, %[slot]\n"
      "v_add_u32 v183, s47, v204\n"
      "v_add_u32 v182, s47, v203\n"
      "v_exp_f32 v105, v89\n"
      "v_exp_f32 v106, v90\n"
      "v_exp_f32 v107, v91\n"
      "v_exp_f32 v108, v92\n"
      "v_exp_f32 v109, v93\n"
      "v_exp_f32 v110, v94\n"
      "v_exp_f32 v111, v95\n"
      "v_pk_add_f32 v[96:97], v[96:97], 1.0 op_sel_hi:[1,0]\n"
      "v_pk_add_f32 v[98:99], v[98:99], 1.0 op_sel_hi:[1,0]\n"
      "v_pk_add_f32 v[100:101], v[100:101], 1.0 op_sel_hi:[1,0]\n"
      "v_pk_add_f32 v[102:103], v[102:103], 1.0 op_sel_hi:[1,0]\n"
      "v_pk_add_f32 v[104:105], v[104:105], 1.0 op_sel_hi:[1,0]\n"
      "v_pk_add_f32 v[106:107], v[106:107], 1.0 op_sel_hi:[1,0]\n"
      "v_pk_add_f32 v[108:109], v[108:109], 1.0 op_sel_hi:[1,0]\n"
      "v_pk_add_f32 v[110:111], v[110:111], 1.0 op_sel_hi:[1,0]\n"
      "v_rcp_f32 v96, v96\n"
      "v_rcp_f32 v97, v97\n"
      "s_mov_b32 exec_lo, 0xff\n"
      "s_mov_b32 exec_hi, 0xff\n"
      "ds_write_b128 v183, v[32:35]\n"
      "ds_write_b128 v183, v[36:39] offset:32\n"
      "ds_write_b128 v183, v[40:43] offset:64\n"
      "ds_write_b128 v183, v[44:47] offset:96\n"
      "s_mov_b64 exec, -1\n"
      "v_rcp_f32 v98, v98\n"
      "v_rcp_f32 v99, v99\n"
      "v_rcp_f32 v100, v100\n"
      "v_rcp_f32 v101, v101\n"
      "v_rcp_f32 v102, v102\n"
      "v_rcp_f32 v103, v103\n"
      "v_rcp_f32 v104, v104\n"
      "v_rcp_f32 v105, v105\n"
      "v_rcp_f32 v106, v106\n"
      "v_rcp_f32 v107, v107\n"
      "v_rcp_f32 v108, v108\n"
      "v_rcp_f32 v109, v109\n"
      "v_rcp_f32 v110, v110\n"
      "v_rcp_f32 v111, v111\n"
      "v_pk_mul_f32 v[96:97], v[80:81], v[96:97]\n"
      "v_pk_mul_f32 v[98:99], v[82:83], v[98:99]\n"
      "v_pk_mul_f32 v[100:101], v[84:85], v[100:101]\n"
      "v_pk_mul_f32 v[102:103], v[86:87], v[102:103]\n"
      "v_pk_mul_f32 v[104:105], v[88:89], v[104:105]\n"
      "v_pk_mul_f32 v[106:107], v[90:91], v[106:107]\n"
      "v_pk_mul_f32 v[108:109], v[92:93], v[108:109]\n"
      "v_pk_mul_f32 v[110:111], v[94:95], v[110:111]\n"
      "v_cvt_pk_bf16_f32 v120, v96, v97\n"
      "v_cvt_pk_bf16_f32 v121, v98, v99\n"
      "v_cvt_pk_bf16_f32 v122, v100, v101\n"
      "v_cvt_pk_bf16_f32 v123, v102, v103\n"
      "v_cvt_pk_bf16_f32 v124, v104, v105\n"
      "v_cvt_pk_bf16_f32 v125, v106, v107\n"
      "v_cvt_pk_bf16_f32 v126, v108, v109\n"
      "v_cvt_pk_bf16_f32 v127, v110, v111\n"
      "s_waitcnt lgkmcnt(4)\n"
      "v_mfma_f32_32x32x16_bf16 v[48:63], v[120:123], v[164:167], v[48:63]\n"
      "v_mfma_f32_32x32x16_bf16 v[48:63], v[124:127], v[168:171], v[48:63]\n"
      "v_pk_mul_f32 v[64:65], v[32:33], v[128:129]\n"
      "v_pk_fma_f32 v[64:65], v[34:35], v[130:131], v[64:65]\n"
      "v_pk_fma_f32 v[64:65], v[36:37], v[132:133], v[64:65]\n"
      "v_pk_fma_f32 v[64:65], v[38:39], v[134:135], v[64:65]\n"
      "v_pk_fma_f32 v[64:65], v[40:41], v[136:137], v[64:65]\n"
      "v_pk_fma_f32 v[64:65], v[42:43], v[138:139], v[64:65]\n"
      "v_pk_fma_f32 v[64:65], v[44:45], v[140:141], v[64:65]\n"
      "v_pk_fma_f32 v[64:65], v[46:47], v[142:143], v[64:65]\n"
      "s_nop 3\n"
      "s_mov_b32 exec_lo, 0xff\n"
      "s_mov_b32 exec_hi, 0xff\n"
      "ds_write_b128 v183, v[48:51] offset:128\n"
      "ds_write_b128 v183, v[52:55] offset:160\n"
      "ds_write_b128 v183, v[56:59] offset:192\n"
      "ds_write_b128 v183, v[60:63] offset:224\n"
      "s_mov_b64 exec, -1\n"
      "v_pk_fma_f32 v[64:65], v[48:49], v[144:145], v[64:65]\n"
      "v_pk_fma_f32 v[64:65], v[50:51], v[146:147], v[64:65]\n"
      "v_pk_fma_f32 v[64:65], v[52:53], v[148:149], v[64:65]\n"
      "v_pk_fma_f32 v[64:65], v[54:55], v[150:151], v[64:65]\n"
      "v_pk_fma_f32 v[64:65], v[56:57], v[152:153], v[64:65]\n"
      "v_pk_fma_f32 v[64:65], v[58:59], v[154:155], v[64:65]\n"
      "v_pk_fma_f32 v[64:65], v[60:61], v[172:173], v[64:65]\n"
      "v_pk_fma_f32 v[64:65], v[62:63], v[174:175], v[64:65]\n"
      "v_add_f32 v64, v64, v65\n"
      "ds_write_b32 v182, v64\n"
      "s_branch L_END_%=\n"
      "L_CWE_%=:\n"
      "s_lshl_b32 s47, s44, 10\n"
      "v_bfi_b32 v183, v194, 0, s47\n"
      "v_add_u32 v183, v183, v201\n"
      "ds_read_b128 v[148:151], v183\n"
      "ds_read_b128 v[152:155], v183 offset:512\n"
      "s_branch L_RWE_%=\n"
      "L_CFE_%=:\n"
      "ds_read_b128 v[64:67], v202\n"
      "ds_read_b128 v[68:71], v202 offset:32\n"
      "ds_read_b128 v[72:75], v202 offset:64\n"
      "ds_read_b128 v[76:79], v202 offset:96\n"
      "ds_read_b128 v[96:99], v202 offset:128\n"
      "ds_read_b128 v[100:103], v202 offset:160\n"
      "ds_read_b128 v[104:107], v202 offset:192\n"
      "ds_read_b128 v[108:111], v202 offset:224\n"
      "s_mul_i32 s47, s46, 2432\n"
      "s_add_u32 s47, s47, %[slot]\n"
      "v_add_u32 v183, s47, v204\n"
      "s_mov_b32 exec_lo, 0xff\n"
      "s_mov_b32 exec_hi, 0xff\n"
      "ds_write_b128 v183, v[32:35]\n"
      "ds_write_b128 v183, v[48:51] offset:128\n"
      "ds_write_b128 v183, v[36:39] offset:32\n"
      "ds_write_b128 v183, v[52:55] offset:160\n"
      "ds_write_b128 v183, v[40:43] offset:64\n"
      "ds_write_b128 v183, v[56:59] offset:192\n"
      "ds_write_b128 v183, v[44:47] offset:96\n"
      "ds_write_b128 v183, v[60:63] offset:224\n"
      "s_mov_b64 exec, -1\n"
      "v_add_u32 v183, s47, v203\n"
      "s_waitcnt lgkmcnt(8)\n"
      "v_pk_mul_f32 v[64:65], v[32:33], v[64:65]\n"
      "v_pk_fma_f32 v[64:65], v[34:35], v[66:67], v[64:65]\n"
      "v_pk_fma_f32 v[64:65], v[36:37], v[68:69], v[64:65]\n"
      "v_pk_fma_f32 v[64:65], v[38:39], v[70:71], v[64:65]\n"
      "v_pk_fma_f32 v[64:65], v[40:41], v[72:73], v[64:65]\n"
      "v_pk_fma_f32 v[64:65], v[42:43], v[74:75], v[64:65]\n"
      "v_pk_fma_f32 v[64:65], v[44:45], v[76:77], v[64:65]\n"
      "v_pk_fma_f32 v[64:65], v[46:47], v[78:79], v[64:65]\n"
      "v_pk_fma_f32 v[64:65], v[48:49], v[96:97], v[64:65]\n"
      "v_pk_fma_f32 v[64:65], v[50:51], v[98:99], v[64:65]\n"
      "v_pk_fma_f32 v[64:65], v[52:53], v[100:101], v[64:65]\n"
      "v_pk_fma_f32 v[64:65], v[54:55], v[102:103], v[64:65]\n"
      "v_pk_fma_f32 v[64:65], v[56:57], v[104:105], v[64:65]\n"
      "v_pk_fma_f32 v[64:65], v[58:59], v[106:107], v[64:65]\n"
      "v_pk_fma_f32 v[64:65], v[60:61], v[108:109], v[64:65]\n"
      "v_pk_fma_f32 v[64:65], v[62:63], v[110:111], v[64:65]\n"
      "v_add_f32 v64, v64, v65\n"
      "ds_write_b32 v183, v64\n"
      "v_mov_b64 v[32:33], 0\n"
      "v_mov_b64 v[34:35], 0\n"
      "v_mov_b64 v[36:37], 0\n"
      "v_mov_b64 v[38:39], 0\n"
      "v_mov_b64 v[40:41], 0\n"
      "v_mov_b64 v[42:43], 0\n"
      "v_mov_b64 v[44:45], 0\n"
      "v_mov_b64 v[46:47], 0\n"
      "v_mov_b64 v[48:49], 0\n"
      "v_mov_b64 v[50:51], 0\n"
      "v_mov_b64 v[52:53], 0\n"
      "v_mov_b64 v[54:55], 0\n"
      "v_mov_b64 v[56:57], 0\n"
      "v_mov_b64 v[58:59], 0\n"
      "v_mov_b64 v[60:61], 0\n"
      "v_mov_b64 v[62:63], 0\n"
      "s_branch L_RFE_%=\n"
      "L_CPE_%=:\n"
      "s_setprio 0\n"
      "s_branch L_RPE_%=\n"
      "L_CWO_%=:\n"
      "s_lshl_b32 s47, s44, 10\n"
      "v_bfi_b32 v183, v194, 0, s47\n"
      "v_add_u32 v183, v183, v201\n"
      "ds_read_b128 v[148:151], v183\n"
      "ds_read_b128 v[152:155], v183 offset:512\n"
      "s_branch L_RWO_%=\n"
      "L_CI_%=:\n"
      "s_mov_b32 s58, s52\n"
      "s_and_b32 s47, s58, 0xf0\n"
      "v_mov_b32 v182, s47\n"
      "s_lshl_b32 s47, s58, 4\n"
      "s_and_b32 s47, s47, 0xf0\n"
      "v_bfi_b32 v182, v194, s47, v182\n"
      "v_sub_u32 v198, v197, v182\n"
      "s_branch L_RI_%=\n"
      "L_CFO_%=:\n"
      "ds_read_b128 v[64:67], v202\n"
      "ds_read_b128 v[68:71], v202 offset:32\n"
      "ds_read_b128 v[72:75], v202 offset:64\n"
      "ds_read_b128 v[76:79], v202 offset:96\n"
      "ds_read_b128 v[96:99], v202 offset:128\n"
      "ds_read_b128 v[100:103], v202 offset:160\n"
      "ds_read_b128 v[104:107], v202 offset:192\n"
      "ds_read_b128 v[108:111], v202 offset:224\n"
      "s_mul_i32 s47, s46, 2432\n"
      "s_add_u32 s47, s47, %[slot]\n"
      "v_add_u32 v183, s47, v204\n"
      "s_mov_b32 exec_lo, 0xff\n"
      "s_mov_b32 exec_hi, 0xff\n"
      "ds_write_b128 v183, v[32:35]\n"
      "ds_write_b128 v183, v[48:51] offset:128\n"
      "ds_write_b128 v183, v[36:39] offset:32\n"
      "ds_write_b128 v183, v[52:55] offset:160\n"
      "ds_write_b128 v183, v[40:43] offset:64\n"
      "ds_write_b128 v183, v[56:59] offset:192\n"
      "ds_write_b128 v183, v[44:47] offset:96\n"
      "ds_write_b128 v183, v[60:63] offset:224\n"
      "s_mov_b64 exec, -1\n"
      "v_add_u32 v183, s47, v203\n"
      "s_waitcnt lgkmcnt(8)\n"
      "v_pk_mul_f32 v[64:65], v[32:33], v[64:65]\n"
      "v_pk_fma_f32 v[64:65], v[34:35], v[66:67], v[64:65]\n"
      "v_pk_fma_f32 v[64:65], v[36:37], v[68:69], v[64:65]\n"
      "v_pk_fma_f32 v[64:65], v[38:39], v[70:71], v[64:65]\n"
      "v_pk_fma_f32 v[64:65], v[40:41], v[72:73], v[64:65]\n"
      "v_pk_fma_f32 v[64:65], v[42:43], v[74:75], v[64:65]\n"
      "v_pk_fma_f32 v[64:65], v[44:45], v[76:77], v[64:65]\n"
      "v_pk_fma_f32 v[64:65], v[46:47], v[78:79], v[64:65]\n"
      "v_pk_fma_f32 v[64:65], v[48:49], v[96:97], v[64:65]\n"
      "v_pk_fma_f32 v[64:65], v[50:51], v[98:99], v[64:65]\n"
      "v_pk_fma_f32 v[64:65], v[52:53], v[100:101], v[64:65]\n"
      "v_pk_fma_f32 v[64:65], v[54:55], v[102:103], v[64:65]\n"
      "v_pk_fma_f32 v[64:65], v[56:57], v[104:105], v[64:65]\n"
      "v_pk_fma_f32 v[64:65], v[58:59], v[106:107], v[64:65]\n"
      "v_pk_fma_f32 v[64:65], v[60:61], v[108:109], v[64:65]\n"
      "v_pk_fma_f32 v[64:65], v[62:63], v[110:111], v[64:65]\n"
      "v_add_f32 v64, v64, v65\n"
      "ds_write_b32 v183, v64\n"
      "v_mov_b64 v[32:33], 0\n"
      "v_mov_b64 v[34:35], 0\n"
      "v_mov_b64 v[36:37], 0\n"
      "v_mov_b64 v[38:39], 0\n"
      "v_mov_b64 v[40:41], 0\n"
      "v_mov_b64 v[42:43], 0\n"
      "v_mov_b64 v[44:45], 0\n"
      "v_mov_b64 v[46:47], 0\n"
      "v_mov_b64 v[48:49], 0\n"
      "v_mov_b64 v[50:51], 0\n"
      "v_mov_b64 v[52:53], 0\n"
      "v_mov_b64 v[54:55], 0\n"
      "v_mov_b64 v[56:57], 0\n"
      "v_mov_b64 v[58:59], 0\n"
      "v_mov_b64 v[60:61], 0\n"
      "v_mov_b64 v[62:63], 0\n"
      "s_branch L_RFO_%=\n"
      "L_CPO_%=:\n"
      "s_setprio 0\n"
      "s_branch L_RPO_%=\n"
      "L_SLOW_%=:\n"
      "s_mov_b32 %[slow], 1\n"
      "s_waitcnt vmcnt(0)\n"
      "s_mov_b64 exec, -1\n"
      "L_END_%=:\n"
      "s_setprio 0\n"
      "s_waitcnt vmcnt(0)\n"
      "s_mov_b32 m0, s48\n"
      : "+{v[196:199]}"(k1), "+{v[164:167]}"(pay), "+{v[156:159]}"(idf0), "+{v[160:163]}"(idf1), "+{v[192:195]}"(k0), "+{v[204:207]}"(k3), "={v[250:253]}"(sa), [slow] "=&s"(slow)
      : "{v[28:31]}"(k5), "{v[200:203]}"(k2), "{v[208:211]}"(k4),
        [w] "s"(w), [rec] "s"(recS), [bp] "s"(bpS), [p4] "s"(p4b), [m0v] "s"(dmaB), [slot] "s"(slotB), [img] "s"(imgB),
        [qrow] "s"(qrow), [einc] "s"(einc), [epub] "s"(epub), [nonce] "s"(nonce), [nb] "s"(__builtin_amdgcn_readfirstlane(b * NPB))
      : "v32", "v33", "v34", "v35", "v36", "v37", "v38", "v39", "v40", "v41", "v42", "v43", "v44", "v45", "v46", "v47",
        "v48", "v49", "v50", "v51", "v52", "v53", "v54", "v55", "v56", "v57", "v58", "v59", "v60", "v61", "v62", "v63",
        "v64", "v65", "v66", "v67", "v68", "v69", "v70", "v71", "v72", "v73", "v74", "v75", "v76", "v77", "v78", "v79",
        "v80", "v81", "v82", "v83", "v84", "v85", "v86", "v87", "v88", "v89", "v90", "v91", "v92", "v93", "v94", "v95",
        "v96", "v97", "v98", "v99", "v100", "v101", "v102", "v103", "v104", "v105", "v106", "v107", "v108", "v109", "v110", "v111",
        "v112", "v113", "v114", "v115", "v116", "v117", "v118", "v119", "v120", "v121", "v122", "v123", "v124", "v125", "v126", "v127",
        "v128", "v129", "v130", "v131", "v132", "v133", "v134", "v135", "v136", "v137", "v138", "v139", "v140", "v141", "v142", "v143",
        "v144", "v145", "v146", "v147", "v148", "v149", "v150", "v151", "v152", "v153", "v154", "v155", "v168", "v169", "v170", "v171",
        "v172", "v173", "v174", "v175", "v176", "v177", "v178", "v179", "v180", "v181", "v182", "v183", "v184", "v185", "v186", "v187",
        "v188", "v189", "v190", "v191", "s40", "s41", "s42", "s43", "s44", "s45", "s46", "s47", "s48", "s49", "s50", "s51",
        "s52", "s53", "s54", "s55", "s56", "s57", "s58", "s59", "s60", "s61", "s62", "s63", "s64", "s65", "s66", "s67",
        "s68", "s69", "s70", "s71", "s72", "s73", "s74", "s75", "s76", "s77", "s78", "s79", "s80", "s81", "s82", "s83",
        "s84", "s85", "s86", "s87", "s88", "s89", "s90", "s91", "s92", "s93", "s94", "s95", "s96", "s97", "vcc", "scc",
        "memory");
  if (slow) {
    slow_path(L, b, w * 64 + k3[1], w, ws, pos, k0[3], idf0, idf1, k0[0]);
    sa = u32x4{k5[1] + (k5[0] >> 3) * SLOT_SZ, L + L_ZERO, L + L_ZERO, 0};
  }

  const u64 vm = w == 7 ? 0x00FFFFFFFFFFFFFFull : (w >> 1) == 2 ? 0x00000000FFFFFFFFull : ~0ull;
  asm volatile(
      "s_waitcnt lgkmcnt(0)\n"
      "s_barrier\n"
      "s_cmp_gt_u32 %[w], 5\n"
      "s_cbranch_scc1 L_POS_%=\n"
      "ds_read_b128 v[32:35], v250\n"
      "ds_read_b128 v[36:39], v250 offset:16\n"
      "ds_read_b128 v[40:43], v250 offset:128\n"
      "ds_read_b128 v[44:47], v250 offset:144\n"
      "ds_read_b128 v[48:51], v251\n"
      "ds_read_b128 v[52:55], v251 offset:16\n"
      "ds_read_b128 v[56:59], v251 offset:128\n"
      "ds_read_b128 v[60:63], v251 offset:144\n"
      "ds_read_b128 v[64:67], v252\n"
      "ds_read_b128 v[68:71], v252 offset:16\n"
      "ds_read_b128 v[72:75], v252 offset:128\n"
      "ds_read_b128 v[76:79], v252 offset:144\n"
      "v_cmp_ne_u32 vcc, 0, v253\n"
      "s_mov_b32 s40, 0xbeb17218\n"
      "s_mov_b32 s42, 0xbfb8aa3b\n"
      "v_lshrrev_b32 v104, 16, v31\n"
      "v_and_b32 v105, 0xffff, v31\n"
      "s_waitcnt lgkmcnt(4)\n"
      "v_pk_add_f32 v[32:33], v[32:33], v[48:49]\n"
      "v_pk_add_f32 v[34:35], v[34:35], v[50:51]\n"
      "v_pk_add_f32 v[36:37], v[36:37], v[52:53]\n"
      "v_pk_add_f32 v[38:39], v[38:39], v[54:55]\n"
      "v_pk_add_f32 v[40:41], v[40:41], v[56:57]\n"
      "v_pk_add_f32 v[42:43], v[42:43], v[58:59]\n"
      "v_pk_add_f32 v[44:45], v[44:45], v[60:61]\n"
      "v_pk_add_f32 v[46:47], v[46:47], v[62:63]\n"
      "s_cbranch_vccz L_E1_%=\n"
      "L_E0_%=:\n"
      "s_mov_b64 exec, vcc\n"
      "v_ffbl_b32 v88, v253\n"
      "s_movk_i32 s41, 2432\n"
      "v_add_u32 v89, -1, v253\n"
      "v_mad_u32_u24 v88, v88, s41, v29\n"
      "v_and_b32 v253, v89, v253\n"
      "ds_read_b128 v[48:51], v88\n"
      "ds_read_b128 v[52:55], v88 offset:16\n"
      "ds_read_b128 v[56:59], v88 offset:128\n"
      "ds_read_b128 v[60:63], v88 offset:144\n"
      "v_cmp_ne_u32 vcc, 0, v253\n"
      "s_waitcnt lgkmcnt(0)\n"
      "v_pk_add_f32 v[32:33], v[32:33], v[48:49]\n"
      "v_pk_add_f32 v[34:35], v[34:35], v[50:51]\n"
      "v_pk_add_f32 v[36:37], v[36:37], v[52:53]\n"
      "v_pk_add_f32 v[38:39], v[38:39], v[54:55]\n"
      "v_pk_add_f32 v[40:41], v[40:41], v[56:57]\n"
      "v_pk_add_f32 v[42:43], v[42:43], v[58:59]\n"
      "v_pk_add_f32 v[44:45], v[44:45], v[60:61]\n"
      "v_pk_add_f32 v[46:47], v[46:47], v[62:63]\n"
      "s_cbranch_vccnz L_E0_%=\n"
      "s_mov_b64 exec, -1\n"
      "L_E1_%=:\n"
      "s_waitcnt lgkmcnt(0)\n"
      "v_pk_add_f32 v[32:33], v[32:33], v[64:65]\n"
      "v_pk_add_f32 v[34:35], v[34:35], v[66:67]\n"
      "v_pk_add_f32 v[36:37], v[36:37], v[68:69]\n"
      "v_pk_add_f32 v[38:39], v[38:39], v[70:71]\n"
      "v_cvt_pk_bf16_f32 v80, v32, v33\n"
      "v_cvt_pk_bf16_f32 v81, v34, v35\n"
      "v_cvt_pk_bf16_f32 v82, v36, v37\n"
      "v_cvt_pk_bf16_f32 v83, v38, v39\n"
      "s_nop 1\n"
      "v_mfma_f32_16x16x32_bf16 v[96:99], v[222:225], v[80:83], 0\n"
      "v_mfma_f32_16x16x32_bf16 v[100:103], v[226:229], v[80:83], 0\n"
      "v_pk_add_f32 v[40:41], v[40:41], v[72:73]\n"
      "v_pk_add_f32 v[42:43], v[42:43], v[74:75]\n"
      "v_pk_add_f32 v[44:45], v[44:45], v[76:77]\n"
      "v_pk_add_f32 v[46:47], v[46:47], v[78:79]\n"
      "v_cvt_pk_bf16_f32 v84, v40, v41\n"
      "v_cvt_pk_bf16_f32 v85, v42, v43\n"
      "v_cvt_pk_bf16_f32 v86, v44, v45\n"
      "v_cvt_pk_bf16_f32 v87, v46, v47\n"
      "s_nop 1\n"
      "v_mfma_f32_16x16x32_bf16 v[96:99], v[230:233], v[84:87], v[96:99]\n"
      "v_mfma_f32_16x16x32_bf16 v[100:103], v[234:237], v[84:87], v[100:103]\n"
      "s_nop 6\n"
      "v_pk_fma_f32 v[238:239], v[96:97], s[40:41], v[238:239] op_sel_hi:[1,0,1]\n"
      "v_pk_fma_f32 v[240:241], v[98:99], s[40:41], v[240:241] op_sel_hi:[1,0,1]\n"
      "v_pk_fma_f32 v[242:243], v[100:101], s[40:41], v[242:243] op_sel_hi:[1,0,1]\n"
      "v_pk_fma_f32 v[244:245], v[102:103], s[40:41], v[244:245] op_sel_hi:[1,0,1]\n"
      "v_pk_mul_f32 v[88:89], v[238:239], s[42:43] op_sel_hi:[1,0]\n"
      "v_pk_mul_f32 v[90:91], v[240:241], s[42:43] op_sel_hi:[1,0]\n"
      "v_pk_mul_f32 v[92:93], v[242:243], s[42:43] op_sel_hi:[1,0]\n"
      "v_pk_mul_f32 v[94:95], v[244:245], s[42:43] op_sel_hi:[1,0]\n"
      "v_exp_f32 v88, v88\n"
      "v_exp_f32 v89, v89\n"
      "v_exp_f32 v90, v90\n"
      "v_exp_f32 v91, v91\n"
      "v_exp_f32 v92, v92\n"
      "v_exp_f32 v93, v93\n"
      "v_exp_f32 v94, v94\n"
      "v_exp_f32 v95, v95\n"
      "v_pk_add_f32 v[88:89], v[88:89], 1.0 op_sel_hi:[1,0]\n"
      "v_pk_add_f32 v[90:91], v[90:91], 1.0 op_sel_hi:[1,0]\n"
      "v_pk_add_f32 v[92:93], v[92:93], 1.0 op_sel_hi:[1,0]\n"
      "v_pk_add_f32 v[94:95], v[94:95], 1.0 op_sel_hi:[1,0]\n"
      "v_rcp_f32 v88, v88\n"
      "v_rcp_f32 v89, v89\n"
      "v_rcp_f32 v90, v90\n"
      "v_rcp_f32 v91, v91\n"
      "v_rcp_f32 v92, v92\n"
      "v_rcp_f32 v93, v93\n"
      "v_rcp_f32 v94, v94\n"
      "v_rcp_f32 v95, v95\n"
      "v_pk_fma_f32 v[88:89], v[238:239], v[88:89], v[212:213]\n"
      "v_pk_fma_f32 v[90:91], v[240:241], v[90:91], v[214:215]\n"
      "v_pk_fma_f32 v[92:93], v[242:243], v[92:93], v[216:217]\n"
      "v_pk_fma_f32 v[94:95], v[244:245], v[94:95], v[218:219]\n"
      "v_cvt_pk_bf16_f32 v32, v88, v89\n"
      "v_cvt_pk_bf16_f32 v33, v90, v91\n"
      "v_cvt_pk_bf16_f32 v34, v92, v93\n"
      "v_cvt_pk_bf16_f32 v35, v94, v95\n"
      "ds_write2_b64 v105, v[32:33], v[34:35] offset1:4\n"
      "ds_read_b128 v[36:39], v104\n"
      "s_mov_b64 exec, %[vm]\n"
      "s_waitcnt lgkmcnt(0)\n"
      "global_store_dwordx4 v30, v[36:39], %[out] sc1\n"
      "s_branch L_FIN_%=\n"
      "L_POS_%=:\n"
      "ds_read2_b32 v[32:33], v250 offset1:32\n"
      "ds_read2_b32 v[34:35], v251 offset1:32\n"
      "ds_read2_b32 v[36:37], v252 offset1:32\n"
      "v_cmp_ne_u32 vcc, 0, v253\n"
      "s_cbranch_vccz L_P3_%=\n"
      "L_P2_%=:\n"
      "s_mov_b64 exec, vcc\n"
      "v_ffbl_b32 v88, v253\n"
      "s_movk_i32 s41, 2432\n"
      "v_add_u32 v89, -1, v253\n"
      "v_mad_u32_u24 v88, v88, s41, v29\n"
      "v_and_b32 v253, v89, v253\n"
      "ds_read2_b32 v[38:39], v88 offset1:32\n"
      "v_cmp_ne_u32 vcc, 0, v253\n"
      "s_waitcnt lgkmcnt(0)\n"
      "v_add_f32 v238, v238, v38\n"
      "v_add_f32 v238, v238, v39\n"
      "s_cbranch_vccnz L_P2_%=\n"
      "L_P3_%=:\n"
      "s_mov_b64 exec, %[vm]\n"
      "s_waitcnt lgkmcnt(0)\n"
      "v_pk_add_f32 v[32:33], v[32:33], v[34:35]\n"
      "v_pk_add_f32 v[32:33], v[32:33], v[36:37]\n"
      "v_add_f32 v238, v238, v32\n"
      "v_add_f32 v238, v238, v33\n"
      "v_cvt_pk_bf16_f32 v32, v238, v238\n"
      "global_store_short v30, v32, %[out] sc1\n"
      "L_FIN_%=:\n"
      "s_mov_b64 exec, -1\n"
      : "+{v[238:241]}"(d0), "+{v[242:245]}"(d1v), "+{v[250:253]}"(sa)
      : "{v[222:237]}"(wm), "{v[212:219]}"(hu), "{v[28:31]}"(k5), [w] "s"(w), [vm] "s"(vm), [out] "s"(out)
      : "v32", "v33", "v34", "v35", "v36", "v37", "v38", "v39", "v40", "v41", "v42", "v43", "v44", "v45", "v46", "v47",
        "v48", "v49", "v50", "v51", "v52", "v53", "v54", "v55", "v56", "v57", "v58", "v59", "v60", "v61", "v62", "v63",
        "v64", "v65", "v66", "v67", "v68", "v69", "v70", "v71", "v72", "v73", "v74", "v75", "v76", "v77", "v78", "v79",
        "v80", "v81", "v82", "v83", "v84", "v85", "v86", "v87", "v88", "v89", "v90", "v91", "v92", "v93", "v94", "v95",
        "v96", "v97", "v98", "v99", "v100", "v101", "v102", "v103", "v104", "v105", "s40", "s41", "s42", "s43", "vcc", "scc",
        "memory");
}

extern "C" void kernel_launch(void* const* d_in, const int* in_sizes, int n_in, void* d_out, int out_size, void* d_ws,
                              size_t ws_size, hipStream_t stream) {
  egnn_fused<<<NBK, 512, 0, stream>>>((const u16*)d_in[0], (const u16*)d_in[1], (const char*)d_in[2], (const char*)d_in[3],
                                      (const char*)d_in[4], (const u16*)d_in[5], (const u16*)d_in[6], (const u16*)d_in[7],
                                      (char*)d_ws, (u16*)d_out);
}
```
